```python
import torch

def _softmax(x, axis=-1):
    return torch.softmax(x, dim=axis)
def _gelu(x):
    return torch.nn.functional.gelu(x, approximate="tanh")
def _silu(x):
    return torch.nn.functional.silu(x)
def _sigmoid(x):
    return torch.sigmoid(x)
def _layernorm(x, axis=-1, eps=1e-5):
    m = x.mean(dim=axis, keepdim=True)
    v = x.var(dim=axis, keepdim=True, unbiased=False)
    return (x - m) / torch.sqrt(v + eps)
def _scatter_add(tgt, idx, src, axis=0):
    return tgt.clone().index_add_(axis, idx, src)
def _segment_sum(src, seg, n):
    out = torch.zeros((n,) + src.shape[1:], dtype=src.dtype)
    return out.index_add_(0, seg, src)
def _pad(x, pw):
    flat = tuple(v for pair in reversed(pw) for v in pair)
    return torch.nn.functional.pad(x, flat)

kernel_name = 'concat_heads__softmax_full_mha__linear_attn_block__prompt'

def setup_inputs():

    _w = (lambda a: (lambda t: t.to(torch.bfloat16) if t.is_floating_point() else t)(torch.as_tensor(a)))
    def _A_setup():
        B, S, D = 1, 2048, 512
        return {
            "x": _w((lambda *s: torch.randn(*s, dtype=torch.bfloat16) * 0.2)(B, S, D)),
            "wqkv": _w((lambda *s: torch.randn(*s, dtype=torch.bfloat16) * 0.2)(D, 3 * D)),
            "wo": _w((lambda *s: torch.randn(*s, dtype=torch.bfloat16) * 0.05)(D, D)),
        }
    _A_in = _A_setup()

    def _B_setup():
        B, S, D = 1, 2048, 512
        return {
            "x": _w((lambda *s: torch.randn(*s, dtype=torch.bfloat16) * 0.05)(B, S, D)),
            "wqkv": _w((lambda *s: torch.randn(*s, dtype=torch.bfloat16) * 1.6)(D, 3 * D)),
            "wo": _w((lambda *s: torch.randn(*s, dtype=torch.bfloat16) * 0.05)(D, D)),
        }
    _B_in = _B_setup()

    return {
        "A_x": _A_in["x"],
        "A_wqkv": _A_in["wqkv"],
        "A_wo": _A_in["wo"],
        "B_x": _B_in["x"],
        "B_wqkv": _B_in["wqkv"],
        "B_wo": _B_in["wo"],
    }

def reference(A_x, A_wqkv, A_wo, B_x, B_wqkv, B_wo):
    def _A_ref(x, wqkv, wo):
        B, S, D = x.shape
        H = 32; Dh = D // H
        qkv = (lambda x, *d: x.permute(*d))((x @ wqkv).reshape(B, S, 3, H, Dh), 2, 0, 3, 1, 4)
        q, k, v = qkv[0], qkv[1], qkv[2]
        scale = Dh ** -0.5
        scores = torch.einsum("bhqd,bhkd->bhqk", q, k) * scale
        p = _softmax(scores, axis=-1)
        o = torch.einsum("bhqk,bhkd->bhqd", p, v)
        return (lambda x, *d: x.permute(*d))(o, 0, 2, 1, 3).reshape(B, S, D) @ wo

    def _B_ref(x, wqkv, wo):
        B, S, D = x.shape
        H = 32; Dh = D // H
        qkv = (lambda x, *d: x.permute(*d))((x @ wqkv).reshape(B, S, 3, H, Dh), 2, 0, 3, 1, 4)
        q, k, v = qkv[0], qkv[1], qkv[2]
        phi = lambda t: torch.where(t > 0, t + 1, torch.exp(t)) + 1e-6
        pq, pk = phi(q), phi(k)
        kv = torch.einsum("bhsd,bhse->bhde", pk, v)
        z = torch.einsum("bhsd->bhd", pk)
        num = torch.einsum("bhqd,bhde->bhqe", pq, kv)
        den = torch.einsum("bhqd,bhd->bhq", pq, z)[..., None] + 1e-6
        o = num / den
        return (lambda x, *d: x.permute(*d))(o, 0, 2, 1, 3).reshape(B, S, D) @ wo

    a = _A_ref(A_x, A_wqkv, A_wo)
    b = _B_ref(B_x, B_wqkv, B_wo)
    return torch.concatenate([a, b], axis=-1)
```

```cpp
#include <hip/hip_runtime.h>
#include <hip/hip_bf16.h>
#include <stdint.h>

typedef __bf16 bf16x8 __attribute__((ext_vector_type(8)));
typedef __bf16 bf16x16 __attribute__((ext_vector_type(16)));
typedef __bf16 bf16x2 __attribute__((ext_vector_type(2)));
typedef short s16x4 __attribute__((ext_vector_type(4)));
typedef short s16x8 __attribute__((ext_vector_type(8)));
typedef float f32x16 __attribute__((ext_vector_type(16)));
typedef float f32x8 __attribute__((ext_vector_type(8)));
typedef float f32x4 __attribute__((ext_vector_type(4)));
typedef float f32x2 __attribute__((ext_vector_type(2)));
typedef unsigned u32x4 __attribute__((ext_vector_type(4)));
typedef unsigned u32x2 __attribute__((ext_vector_type(2)));
#define LAS __attribute__((address_space(3)))
typedef LAS char* lds_ptr;

constexpr size_t WS_QA = 0, WS_KV = 2u << 20, WS_PQ = 6u << 20, WS_OA = 8u << 20, WS_OB = 10u << 20, WS_KVP = 12u << 20;

#define MFMA(a, b, c) __builtin_amdgcn_mfma_f32_32x32x16_bf16(a, b, c, 0, 0, 0)
#define SMFMAC(a, b, c, idx) __builtin_amdgcn_smfmac_f32_32x32x32_bf16(a, b, c, idx, 0, 0)
#define WAIT_BAR(N) asm volatile("s_waitcnt vmcnt(" #N ") lgkmcnt(0)\n\ts_barrier" ::: "memory")

__device__ __forceinline__ void glds16(const void* g, unsigned lds_base) {
    unsigned sv;
    asm volatile("s_mov_b32 %0, m0\n\ts_mov_b32 m0, %2\n\ts_nop 0\n\tglobal_load_lds_dwordx4 %1, off\n\ts_mov_b32 m0, %0" : "=&s"(sv) : "v"(g), "s"(lds_base) : "memory");
}
__device__ __forceinline__ bf16x8 lds_b128(lds_ptr p) { return *(const LAS bf16x8*)p; }
__device__ __forceinline__ s16x4 lds_tr(lds_ptr p) { return __builtin_amdgcn_ds_read_tr16_b64_v4i16((LAS s16x4*)p); }
__device__ __forceinline__ bf16x8 tr_frag(lds_ptr p0, lds_ptr p1) {
    s16x4 a = lds_tr(p0), b = lds_tr(p1);
    return __builtin_bit_cast(bf16x8, __builtin_shufflevector(a, b, 0, 1, 2, 3, 4, 5, 6, 7));
}
__device__ __forceinline__ unsigned pk2(float lo, float hi) { f32x2 v = {lo, hi}; return __builtin_bit_cast(unsigned, __builtin_convertvector(v, bf16x2)); }
template <int O> __device__ __forceinline__ u32x4 pack8(const f32x16& a) { return u32x4{pk2(a[O], a[O + 1]), pk2(a[O + 2], a[O + 3]), pk2(a[O + 4], a[O + 5]), pk2(a[O + 6], a[O + 7])}; }
__device__ __forceinline__ float bf16r(float x) { return (float)(__bf16)x; }
__device__ __forceinline__ float phi(float t) { t = bf16r(t); return (t > 0.f ? t + 1.f : __builtin_amdgcn_exp2f(t * 1.4426950408889634f)) + 1e-6f; }

template <bool BR>
__device__ __forceinline__ void qkv_body(const char* x, const char* w, char* ws, int rb, int hq, char* smem) {
    const int tid = threadIdx.x, lane = tid & 63, wid = __builtin_amdgcn_readfirstlane(tid >> 6);
    const unsigned lds0 = (unsigned)(uintptr_t)smem;
    lds_ptr L = (lds_ptr)smem;
    constexpr int STG = 40960;
    const int r8 = lane >> 3, c8 = lane & 7;
    const char* xs0 = x + (size_t)(rb * 128 + wid * 16 + r8) * 1024 + ((c8 ^ (r8 >> 1)) * 16);
    const char* xs1 = x + (size_t)(rb * 128 + wid * 16 + 8 + r8) * 1024 + ((c8 ^ (4 + (r8 >> 1))) * 16);
    const char* wsrc = w + (size_t)(wid * 8 + r8) * 3072 + hq * 128 + ((c8 ^ (((r8 >> 1) & 1) << 2)) * 16);
    auto issue = [&](int st) {
        const unsigned b = lds0 + (st & 3) * STG;
        glds16(xs0 + st * 128, b + wid * 2048);
        glds16(xs1 + st * 128, b + wid * 2048 + 1024);
#pragma unroll
        for (int sg = 0; sg < 3; ++sg) glds16(wsrc + (size_t)st * 64 * 3072 + sg * 1024, b + 16384 + (sg * 8 + wid) * 1024);
    };
    issue(0); issue(1); issue(2); issue(3);
    const int t = wid & 3, g = wid >> 2, tau = lane & 31, hi = lane >> 5;
    int xoff[4];
#pragma unroll
    for (int s = 0; s < 4; ++s) xoff[s] = (t * 32 + tau) * 128 + (((2 * s + hi) ^ ((tau >> 1) & 7)) * 16);
    const int q4 = (lane & 15) >> 2, p4 = lane & 3, blk = (lane >> 4) & 1;
    const int woff = 16384 + hi * 1024 + q4 * 128 + (((4 * g + 2 * blk + (p4 >> 1)) ^ ((q4 >> 1) << 2)) * 16) + 8 * (p4 & 1);
    f32x16 aq = {}, ak = {}, av = {};
#pragma unroll
    for (int st = 0; st < 8; ++st) {
        if (st <= 4) { WAIT_BAR(15); } else if (st == 5) { WAIT_BAR(10); } else if (st == 6) { WAIT_BAR(5); } else { WAIT_BAR(0); }
        lds_ptr B = L + (st & 3) * STG;
#pragma unroll
        for (int s = 0; s < 4; ++s) {
            bf16x8 xf = lds_b128(B + xoff[s]);
            bf16x8 wq = tr_frag(B + woff + s * 2048, B + woff + s * 2048 + 512);
            bf16x8 wk = tr_frag(B + woff + 8192 + s * 2048, B + woff + 8192 + s * 2048 + 512);
            bf16x8 wv = tr_frag(B + woff + 16384 + s * 2048, B + woff + 16384 + s * 2048 + 512);
            aq = MFMA(wq, xf, aq);
            if (BR) ak = MFMA(xf, wk, ak); else ak = MFMA(wk, xf, ak);
            av = MFMA(xf, wv, av);
        }
        if (st < 4) { asm volatile("s_waitcnt lgkmcnt(0)\n\ts_barrier" ::: "memory"); issue(st + 4); }
    }
    const int h0 = hq * 4 + g * 2, qt = rb * 4 + t;
    if (!BR) {
        constexpr float C = 0.25f * 1.4426950408889634f;
        aq *= C;
        char* qa = ws + WS_QA + (size_t)(h0 * 64 + qt) * 1024 + lane * 16;
        *(u32x4*)qa = pack8<0>(aq); *(u32x4*)(qa + 65536) = pack8<8>(aq);
        const int rho = (tau & 19) | ((tau & 4) << 1) | ((tau & 8) >> 1);
        char* kk = ws + WS_KV + (size_t)(h0 * 64 + qt) * 2048 + (rho + 32 * hi) * 16;
        *(u32x4*)kk = pack8<0>(ak); *(u32x4*)(kk + 131072) = pack8<8>(ak);
        char* vv = ws + WS_KV + (size_t)((h0 + (tau >> 4)) * 64 + qt) * 2048 + 1024 + hi * 256 + (tau & 15) * 16;
        *(u32x4*)vv = pack8<0>(av); *(u32x4*)(vv + 512) = pack8<8>(av);
    } else {
#pragma unroll
        for (int r = 0; r < 16; ++r) { aq[r] = phi(aq[r]); ak[r] = bf16r(phi(ak[r])); }
        char* pq = ws + WS_PQ + (size_t)(qt * 32 + h0) * 1024 + lane * 16;
        *(u32x4*)pq = pack8<0>(aq); *(u32x4*)(pq + 1024) = pack8<8>(aq);
        float z = 0.f;
#pragma unroll
        for (int r = 0; r < 16; ++r) z += ak[r];
        f32x16 kv = {};
        kv = MFMA(__builtin_bit_cast(bf16x8, pack8<0>(ak)), __builtin_bit_cast(bf16x8, pack8<0>(av)), kv);
        kv = MFMA(__builtin_bit_cast(bf16x8, pack8<8>(ak)), __builtin_bit_cast(bf16x8, pack8<8>(av)), kv);
        asm volatile("s_waitcnt lgkmcnt(0)\n\ts_barrier" ::: "memory");
        LAS float* red = (LAS float*)(L + wid * 2560);
        f32x4 lo, up;
#pragma unroll
        for (int j = 0; j < 4; ++j) { lo[j] = tau < 16 ? kv[j] : kv[8 + j]; up[j] = tau < 16 ? kv[4 + j] : kv[12 + j]; }
        *(LAS f32x4*)(red + lane * 8) = lo; *(LAS f32x4*)(red + lane * 8 + 4) = up;
        red[512 + lane] = z;
        asm volatile("s_waitcnt lgkmcnt(0)\n\ts_barrier" ::: "memory");
        float* kvp = (float*)(ws + WS_KVP);
        for (int i = tid; i < 4 * 272; i += 512) {
            const int hh = i / 272, e = i - hh * 272, gg = hh >> 1, h1 = hh & 1;
            float s = 0.f;
            if (e < 256) { const int ha = e >> 7, ee = (e >> 3) & 15, j = e & 7, ln = ha * 32 + h1 * 16 + ee;
#pragma unroll
                for (int tt = 0; tt < 4; ++tt) s += ((LAS float*)(L + (gg * 4 + tt) * 2560))[ln * 8 + j];
            } else { const int sl = e - 256, ha = sl >> 3, j = sl & 7, d = (j & 3) + 8 * (j >> 2) + 4 * ha;
#pragma unroll
                for (int tt = 0; tt < 4; ++tt) { LAS float* rr = (LAS float*)(L + (gg * 4 + tt) * 2560) + 512; s += rr[h1 * 16 + d] + rr[32 + h1 * 16 + d]; }
            }
            kvp[((hq * 4 + hh) * 16 + rb) * 272 + e] = s;
        }
    }
}
__global__ __launch_bounds__(512, 2) void k_qkv(const char* xa, const char* wa, const char* xb, const char* wb, char* ws) {
    extern __shared__ __attribute__((aligned(16))) char smem[];
    const int b = blockIdx.x, xcd = b & 7, idx = b >> 3;
    const int rb = (xcd & 1) * 8 + (idx & 7), hq = ((xcd >> 1) & 1) * 4 + (idx >> 3);
    if (xcd >> 2) qkv_body<true>(xb, wb, ws, rb, hq, smem); else qkv_body<false>(xa, wa, ws, rb, hq, smem);
}

constexpr int ONES = 131072, RED = ONES + 4096, K2_LDS = RED + 1152;
__global__ __launch_bounds__(512, 2) void k_attn(char* ws) {
    extern __shared__ __attribute__((aligned(16))) char smem[];
    const int tid = threadIdx.x, lane = tid & 63, wid = __builtin_amdgcn_readfirstlane(tid >> 6);
    const int b = blockIdx.x, xcd = b & 7, idx = b >> 3, h = xcd * 4 + (idx >> 3), qt = (idx & 7) * 8 + wid;
    const unsigned lds0 = (unsigned)(uintptr_t)smem;
    lds_ptr L = (lds_ptr)smem;
    const char* kvs = ws + WS_KV + (size_t)h * 131072 + wid * 2048 + lane * 16;
    const bf16x8 qf = *(const bf16x8*)(ws + WS_QA + (size_t)(h * 64 + qt) * 1024 + lane * 16);
    const bf16x8 pqf = *(const bf16x8*)(ws + WS_PQ + (size_t)(qt * 32 + h) * 1024 + lane * 16);
    float kvsum = 0.f;
    if (tid < 272) { const float* kvp = (const float*)(ws + WS_KVP) + h * 16 * 272 + tid;
#pragma unroll
        for (int i = 0; i < 16; ++i) kvsum += kvp[i * 272]; }
    asm volatile("" : "+v"(kvsum));
#pragma unroll
    for (int c = 0; c < 8; ++c) { glds16(kvs + c * 16384, lds0 + c * 16384 + wid * 2048); glds16(kvs + c * 16384 + 1024, lds0 + c * 16384 + wid * 2048 + 1024); }
    *(LAS u32x2*)(L + ONES + tid * 8) = u32x2{0x3f803f80u, 0x3f803f80u};
    if (tid < 272) ((LAS float*)(L + RED))[tid] = kvsum;
    WAIT_BAR(14);
    const int r = lane & 31, hi = lane >> 5;
    {
        const LAS float* src = (const LAS float*)(L + RED) + (r < 16 ? (hi * 16 + r) * 8 : 256 + hi * 8);
        const f32x4 a0 = *(const LAS f32x4*)src, a1 = *(const LAS f32x4*)(src + 4);
        const u32x4 af = {pk2(a0[0], a0[1]), pk2(a0[2], a0[3]), pk2(a1[0], a1[1]), pk2(a1[2], a1[3])};
        f32x16 nd = {};
        nd = MFMA(__builtin_bit_cast(bf16x8, af), pqf, nd);
        const float inv = 1.f / (nd[8] + 1e-6f);
        nd *= inv;
        *(u32x4*)(ws + WS_OB + (size_t)(qt * 32 + h) * 1024 + lane * 16) = pack8<0>(nd);
    }
    int idxv = 0xdddddddd; asm volatile("" : "+v"(idxv));
    lds_ptr kp = L + lane * 16;
    lds_ptr vp = r < 16 ? L + 1024 + hi * 256 + r * 16 : L + ONES + (r & 15) * 16;
    const int vstep = r < 16 ? 4096 : 0;
    f32x16 o = {};
    const f32x16 zero = {};
#define PLO(s) __builtin_bit_cast(bf16x16, __builtin_shufflevector(s, s, 0, 1, 2, 3, 4, 5, 6, 7))
#define PHI(s) __builtin_bit_cast(bf16x16, __builtin_shufflevector(s, s, 8, 9, 10, 11, 12, 13, 14, 15))
    f32x16 sA = MFMA(lds_b128(kp), qf, zero), sB;
    for (int t = 0; t < 64; t += 2) {
        if (t == 6) { WAIT_BAR(0); }
        const bf16x8 k1 = lds_b128(kp + 2048), v0 = lds_b128(vp), v1 = lds_b128(vp + 512);
        sB = MFMA(k1, qf, zero);
#pragma unroll
        for (int i = 0; i < 16; ++i) sA[i] = __builtin_amdgcn_exp2f(sA[i]);
        o = SMFMAC(v0, PLO(sA), o, idxv); o = SMFMAC(v1, PHI(sA), o, idxv);
        const bf16x8 k2 = lds_b128(kp + 4096), v2 = lds_b128(vp + 2048), v3 = lds_b128(vp + 2048 + 512);
        sA = MFMA(k2, qf, zero);
#pragma unroll
        for (int i = 0; i < 16; ++i) sB[i] = __builtin_amdgcn_exp2f(sB[i]);
        o = SMFMAC(v2, PLO(sB), o, idxv); o = SMFMAC(v3, PHI(sB), o, idxv);
        kp += 4096; vp += vstep;
    }
    bool bad = !(o[8] >= 5.9604645e-8f && o[8] <= 1.8446744e19f);
#pragma unroll
    for (int i = 0; i < 8; ++i) bad |= !(__builtin_fabsf(o[i]) < 3.0e38f);
    if (__builtin_expect(__any(bad), 0)) {
        kp = L + lane * 16; vp = r < 16 ? L + 1024 + hi * 256 + r * 16 : L + ONES + (r & 15) * 16;
        float m = -3.0e38f; o = zero;
        for (int t = 0; t < 64; ++t) {
            f32x16 s = MFMA(lds_b128(kp), qf, zero);
            float mx = m;
#pragma unroll
            for (int i = 0; i < 16; ++i) mx = __builtin_fmaxf(mx, s[i]);
            auto sw = __builtin_amdgcn_permlane32_swap(__float_as_uint(mx), __float_as_uint(mx), false, false);
            mx = __builtin_fmaxf(__uint_as_float(sw[0]), __uint_as_float(sw[1]));
            o *= __builtin_amdgcn_exp2f(m - mx); m = mx;
#pragma unroll
            for (int i = 0; i < 16; ++i) s[i] = __builtin_amdgcn_exp2f(s[i] - mx);
            o = SMFMAC(lds_b128(vp), PLO(s), o, idxv); o = SMFMAC(lds_b128(vp + 512), PHI(s), o, idxv);
            kp += 2048; vp += vstep >> 1;
        }
    }
    o *= 1.f / o[8];
    *(u32x4*)(ws + WS_OA + (size_t)(qt * 32 + h) * 1024 + lane * 16) = pack8<0>(o);
}

__global__ __launch_bounds__(512, 2) void k_out(const char* woa, const char* wob, const char* ws, char* out) {
    extern __shared__ __attribute__((aligned(16))) char smem[];
    const int tid = threadIdx.x, lane = tid & 63, wid = __builtin_amdgcn_readfirstlane(tid >> 6);
    const int b = blockIdx.x, xcd = b & 7, idx = b >> 3, br = xcd >> 2, rbk = (xcd & 3) * 8 + (idx >> 2), cb = idx & 3;
    const unsigned lds0 = (unsigned)(uintptr_t)smem;
    lds_ptr L = (lds_ptr)smem;
    constexpr int STG = 49152;
    const char* osrc = ws + (br ? WS_OB : WS_OA) + (size_t)(rbk * 2) * 32768 + wid * 1024 + lane * 16;
    const int r4 = lane >> 4, c16 = lane & 15;
    const char* wsrc = (br ? wob : woa) + (size_t)(wid * 16 + r4) * 1024 + cb * 256 + ((c16 ^ (r4 << 2)) * 16);
    auto issue = [&](int st) {
        const unsigned bb = lds0 + (st % 3) * STG;
        glds16(osrc + st * 8192, bb + wid * 1024); glds16(osrc + 32768 + st * 8192, bb + 8192 + wid * 1024);
#pragma unroll
        for (int i = 0; i < 4; ++i) glds16(wsrc + (size_t)(st * 128 + i * 4) * 1024, bb + 16384 + (wid * 4 + i) * 1024);
    };
    issue(0); issue(1); issue(2);
    const int it = wid & 1, ct = wid >> 1, hi = lane >> 5;
    const int q4 = (lane & 15) >> 2, p4 = lane & 3, blk = (lane >> 4) & 1;
    const int woff = 16384 + (4 * hi + q4) * 256 + (((4 * ct + 2 * blk + (p4 >> 1)) ^ (q4 << 2)) * 16) + 8 * (p4 & 1);
    f32x16 acc = {};
#pragma unroll
    for (int st = 0; st < 4; ++st) {
        if (st <= 1) { WAIT_BAR(12); } else if (st == 2) { WAIT_BAR(6); } else { WAIT_BAR(0); }
        lds_ptr B = L + (st % 3) * STG;
#pragma unroll
        for (int hh = 0; hh < 8; ++hh) {
            const bf16x8 of = lds_b128(B + it * 8192 + hh * 1024 + lane * 16);
            const bf16x8 wf = tr_frag(B + woff + hh * 4096, B + woff + hh * 4096 + 2048);
            acc = MFMA(wf, of, acc);
        }
        if (st == 0) { asm volatile("s_waitcnt lgkmcnt(0)\n\ts_barrier" ::: "memory"); issue(3); }
    }
    lds_ptr T = L + STG;
    const int tok = it * 32 + (lane & 31);
#pragma unroll
    for (int q = 0; q < 4; ++q) *(LAS u32x2*)(T + tok * 272 + ct * 64 + q * 16 + hi * 8) = u32x2{pk2(acc[4 * q], acc[4 * q + 1]), pk2(acc[4 * q + 2], acc[4 * q + 3])};
    asm volatile("s_waitcnt lgkmcnt(0)\n\ts_barrier" ::: "memory");
#pragma unroll
    for (int i = 0; i < 2; ++i) { const int row = i * 32 + (tid >> 4), ch = tid & 15;
        *(u32x4*)(out + (size_t)(rbk * 64 + row) * 2048 + br * 1024 + cb * 256 + ch * 16) = *(const LAS u32x4*)(T + row * 272 + ch * 16); }
}

extern "C" void kernel_launch(void* const* d_in, const int* in_sizes, int n_in, void* d_out, int out_size, void* d_ws, size_t ws_size, hipStream_t stream) {
    (void)hipFuncSetAttribute((const void*)k_qkv, hipFuncAttributeMaxDynamicSharedMemorySize, 163840);
    (void)hipFuncSetAttribute((const void*)k_attn, hipFuncAttributeMaxDynamicSharedMemorySize, K2_LDS);
    (void)hipFuncSetAttribute((const void*)k_out, hipFuncAttributeMaxDynamicSharedMemorySize, 147456);
    k_qkv<<<256, 512, 163840, stream>>>((const char*)d_in[0], (const char*)d_in[1], (const char*)d_in[3], (const char*)d_in[4], (char*)d_ws);
    k_attn<<<256, 512, K2_LDS, stream>>>((char*)d_ws);
    k_out<<<256, 512, 147456, stream>>>((const char*)d_in[2], (const char*)d_in[5], (const char*)d_ws, (char*)d_out);
}
```

```cpp
#include <hip/hip_runtime.h>
#include <hip/hip_bf16.h>
#include <stdint.h>

__global__ __launch_bounds__(512, 2) void fused_attn_block(const void* xa, const void* wa, const void* woa, const void* xb, const void* wb, const void* wob, void* out, void* ws) {
    asm volatile(
        "L_START_%=:\n"
        "s_load_dwordx16 s[4:19], %0, 0x0\n"
        "v_lshrrev_b32_e64 v2, 6, %2\n"
        "v_and_b32 v1, 63, %2\n"
        "v_readfirstlane_b32 s60, v2\n"
        "s_and_b32 s68, %1, 1\n"
        "s_lshl_b32 s68, s68, 4\n"
        "s_bfe_u32 s69, %1, 0x40003\n"
        "s_or_b32 s61, s68, s69\n"
        "s_and_b32 s68, %1, 6\n"
        "s_lshr_b32 s69, %1, 7\n"
        "s_or_b32 s62, s68, s69\n"
        "s_lshl_b32 s73, s62, 7\n"
        "v_lshrrev_b32 v16, 3, v1\n"
        "v_and_b32 v17, 7, v1\n"
        "v_lshrrev_b32 v18, 4, v1\n"
        "v_and_b32 v19, 15, v1\n"
        "s_cmp_lt_u32 s60, 4\n"
        "s_cbranch_scc1 L_KVROLE_%=\n"
        "s_setprio 1\n"
        "s_lshl_b32 s68, s62, 1\n"
        "s_add_u32 s68, s68, s61\n"
        "s_and_b32 s68, s68, 7\n"
        "s_lshl_b32 s63, s68, 0\n"
        "s_lshl_b32 s34, s61, 16\n"
        "s_and_b32 s68, s60, 3\n"
        "s_lshl_b32 s33, s68, 11\n"
        "s_lshl_b32 s68, s68, 4\n"
        "v_add_u32 v20, s68, v16\n"
        "v_xor_b32 v21, v17, v18\n"
        "v_lshlrev_b32_e64 v21, 4, v21\n"
        "v_lshl_add_u32 v240, v20, 10, v21\n"
        "v_xor_b32_e64 v241, 64, v240\n"
        "v_add_u32 v241, 0x2000, v241\n"
        "v_and_b32 v21, 2, v16\n"
        "v_xor_b32 v21, v21, v17\n"
        "v_lshl_add_u32 v21, v21, 4, s73\n"
        "v_mul_u32_u24 v242, 0xc00, v20\n"
        "v_add_u32 v242, v242, v21\n"
        "v_xor_b32 v243, 64, v242\n"
        "v_add_u32 v243, 0x6000, v243\n"
        "s_mov_b32 s22, 0x200000\n"
        "s_mov_b32 s23, 0x00020000\n"
        "s_mov_b32 s26, 0x180000\n"
        "s_mov_b32 s27, 0x00020000\n"
        "s_lshl_b32 s35, s63, 7\n"
        "s_add_u32 s35, s35, s34\n"
        "s_mul_i32 s3, s63, 0x30000\n"
        "s_add_u32 s63, s63, 1\n"
        "s_and_b32 s63, s63, 7\n"
        "s_waitcnt lgkmcnt(0)\n"
        "s_mov_b32 s20, s4\n"
        "s_and_b32 s21, s5, 0xffff\n"
        "s_mov_b32 s24, s6\n"
        "s_and_b32 s25, s7, 0xffff\n"
        "s_add_u32 m0, s33, 0x0\n"
        "s_nop 0\n"
        "buffer_load_dwordx4 v240, s[20:23], s35 offen lds\n"
        "s_add_u32 m0, s33, 0x400\n"
        "s_nop 0\n"
        "buffer_load_dwordx4 v241, s[20:23], s35 offen lds\n"
        "s_add_u32 m0, s33, 0x2000\n"
        "s_nop 0\n"
        "buffer_load_dwordx4 v242, s[24:27], s3 offen offset:0 lds\n"
        "s_add_u32 m0, s33, 0x2400\n"
        "s_nop 0\n"
        "buffer_load_dwordx4 v243, s[24:27], s3 offen offset:0 lds\n"
        "s_add_u32 m0, s33, 0x3c00\n"
        "s_nop 0\n"
        "buffer_load_dwordx4 v242, s[24:27], s3 offen offset:1024 lds\n"
        "s_add_u32 m0, s33, 0x4000\n"
        "s_nop 0\n"
        "buffer_load_dwordx4 v243, s[24:27], s3 offen offset:1024 lds\n"
        "s_add_u32 m0, s33, 0x5800\n"
        "s_nop 0\n"
        "buffer_load_dwordx4 v242, s[24:27], s3 offen offset:2048 lds\n"
        "s_add_u32 m0, s33, 0x5c00\n"
        "s_nop 0\n"
        "buffer_load_dwordx4 v243, s[24:27], s3 offen offset:2048 lds\n"
        "s_mov_b32 s52, s18\n"
        "s_and_b32 s53, s19, 0xffff\n"
        "s_mov_b32 s54, 0x1000000\n"
        "s_mov_b32 s55, 0x00020000\n"
        "s_mov_b32 s40, s8\n"
        "s_and_b32 s41, s9, 0xffff\n"
        "s_mov_b32 s42, 0x80000\n"
        "s_mov_b32 s43, 0x00020000\n"
        "s_mov_b32 s44, s14\n"
        "s_and_b32 s45, s15, 0xffff\n"
        "s_mov_b32 s46, 0x80000\n"
        "s_mov_b32 s47, 0x00020000\n"
        "s_mov_b32 s48, s16\n"
        "s_and_b32 s49, s17, 0xffff\n"
        "s_mov_b32 s50, 0x400000\n"
        "s_mov_b32 s51, 0x00020000\n"
        "s_mov_b32 s28, s10\n"
        "s_and_b32 s29, s11, 0xffff\n"
        "s_mov_b32 s30, 0x200000\n"
        "s_mov_b32 s31, 0x00020000\n"
        "s_mov_b32 s36, s12\n"
        "s_and_b32 s37, s13, 0xffff\n"
        "s_mov_b32 s38, 0x180000\n"
        "s_mov_b32 s39, 0x00020000\n"
        "s_mov_b32 s66, 0xffff\n"
        "s_mov_b32 s67, 0xffff\n"
        "s_mov_b32 s72, 0x40000\n"
        "s_and_b32 s68, s60, 3\n"
        "s_lshl_b32 s69, s62, 2\n"
        "s_add_u32 s70, s68, s69\n"
        "s_lshl_b32 s68, s70, 17\n"
        "s_lshr_b32 s71, s60, 2\n"
        "s_lshl_b32 s69, s71, 16\n"
        "s_add_u32 s68, s68, s69\n"
        "s_add_u32 s68, s68, 0x100000\n"
        "v_lshl_add_u32 v11, v1, 4, s68\n"
        "v_lshlrev_b32 v20, 4, v19\n"
        "v_lshrrev_b32_e64 v21, 5, v1\n"
        "v_lshl_add_u32 v20, v21, 8, v20\n"
        "s_add_u32 s68, s68, 0x400\n"
        "v_add_u32 v12, s68, v20\n"
        "s_and_b32 s68, s61, 0\n"
        "s_lshl_b32 s78, s68, 13\n"
        "s_lshl_b32 s68, s61, 1\n"
        "s_add_u32 s68, s68, s71\n"
        "s_lshl_b32 s68, s68, 5\n"
        "s_add_u32 s68, s68, s70\n"
        "s_lshl_b32 s68, s68, 10\n"
        "v_lshl_add_u32 v255, v1, 4, s68\n"
        "s_and_b32 s68, s60, 3\n"
        "s_lshl_b32 s68, s68, 13\n"
        "s_xor_b32 s69, s73, 384\n"
        "s_add_u32 s68, s68, s69\n"
        "v_and_b32 v21, 6, v16\n"
        "v_xor_b32_e64 v21, v21, v17\n"
        "v_lshl_add_u32 v21, v21, 4, s68\n"
        "v_lshl_add_u32 v254, v16, 10, v21\n"
        "s_and_b32 s68, s60, 3\n"
        "s_lshl_b32 s68, s68, 11\n"
        "s_add_u32 s68, s68, 0x20000\n"
        "s_lshr_b32 s69, s60, 2\n"
        "s_lshl_b32 s69, s69, 10\n"
        "s_add_u32 s68, s68, s69\n"
        "v_lshl_add_u32 v226, v1, 4, s68\n"
        "v_xor_b32 v227, 0x400, v226\n"
        "v_and_b32 v21, 27, v1\n"
        "v_cmp_eq_u32 vcc, 16, v21\n"
        "v_mov_b32 v22, 0x3f803f80\n"
        "v_mov_b32 v10, 0xdddddddd\n"
        "v_cndmask_b32 v21, 0, v22, vcc\n"
        "v_mov_b32 v132, v21\n"
        "v_mov_b32 v133, v21\n"
        "v_mov_b32 v134, v21\n"
        "v_mov_b32 v135, v21\n"
        "v_mov_b32 v136, v21\n"
        "v_mov_b32 v137, v21\n"
        "v_mov_b32 v138, v21\n"
        "v_mov_b32 v139, v21\n"
        "v_mov_b32 v144, v21\n"
        "v_mov_b32 v145, v21\n"
        "v_mov_b32 v146, v21\n"
        "v_mov_b32 v147, v21\n"
        "v_mov_b32 v148, v21\n"
        "v_mov_b32 v149, v21\n"
        "v_mov_b32 v150, v21\n"
        "v_mov_b32 v151, v21\n"
        "v_mov_b32 v156, v21\n"
        "v_mov_b32 v157, v21\n"
        "v_mov_b32 v158, v21\n"
        "v_mov_b32 v159, v21\n"
        "v_mov_b32 v160, v21\n"
        "v_mov_b32 v161, v21\n"
        "v_mov_b32 v162, v21\n"
        "v_mov_b32 v163, v21\n"
        "v_mov_b32 v168, v21\n"
        "v_mov_b32 v169, v21\n"
        "v_mov_b32 v170, v21\n"
        "v_mov_b32 v171, v21\n"
        "v_mov_b32 v172, v21\n"
        "v_mov_b32 v173, v21\n"
        "v_mov_b32_e64 v174, v21\n"
        "v_mov_b32 v175, v21\n"
        "s_lshl_b32 s68, s62, 8\n"
        "s_add_u32 s68, s68, 0x1000\n"
        "v_and_b32_e64 v21, 31, v1\n"
        "v_lshl_add_u32 v14, v21, 3, s68\n"
        "s_lshl_b32 s69, s61, 3\n"
        "s_add_u32 s7, s68, s69\n"
        "v_mov_b32 v184, 0\n"
        "v_mov_b32 v185, 0\n"
        "v_mov_b32 v186, 0\n"
        "v_mov_b32 v187, 0\n"
        "v_mov_b32 v188, 0\n"
        "v_mov_b32 v189, 0\n"
        "v_mov_b32 v190, 0\n"
        "v_mov_b32 v191, 0\n"
        "v_mov_b32 v192, 0\n"
        "v_mov_b32 v193, 0\n"
        "v_mov_b32 v194, 0\n"
        "v_mov_b32 v195, 0\n"
        "v_mov_b32 v196, 0\n"
        "v_mov_b32 v197, 0\n"
        "v_mov_b32 v198, 0\n"
        "v_mov_b32 v199, 0\n"
        "v_mov_b32 v200, 0\n"
        "v_mov_b32 v201, 0\n"
        "v_mov_b32 v202, 0\n"
        "v_mov_b32 v203, 0\n"
        "v_mov_b32 v204, 0\n"
        "v_mov_b32 v205, 0\n"
        "v_mov_b32 v206, 0\n"
        "v_mov_b32 v207, 0\n"
        "v_mov_b32 v208, 0\n"
        "v_mov_b32 v209, 0\n"
        "v_mov_b32 v210, 0\n"
        "v_mov_b32 v211, 0\n"
        "v_mov_b32 v212, 0\n"
        "v_mov_b32 v213, 0\n"
        "v_mov_b32 v214, 0\n"
        "v_mov_b32 v215, 0\n"
        "s_mov_b32 s14, 0x701000\n"
        "s_bitcmp1_b32 s62, 0\n"
        "s_cselect_b32 s14, 0x6ff000, s14\n"
        "s_lshl_b32 s18, s62, 0\n"
        "s_lshr_b32 s68, s60, 2\n"
        "s_add_u32 s68, s68, s18\n"
        "s_and_b32 s69, s68, 3\n"
        "s_lshl_b32 s12, s69, 15\n"
        "s_xor_b32 s13, s12, 0x10000\n"
        "s_and_b32 s76, s61, 7\n"
        "s_lshr_b32 s68, s60, 1\n"
        "s_lshl_b32 s70, s68, 14\n"
        "v_bfe_u32 v48, v1, 2, 3\n"
        "v_and_b32 v49, 6, v48\n"
        "v_bfe_u32 v50, v1, 1, 1\n"
        "v_or_b32 v49, v49, v50\n"
        "v_and_b32 v50, 1, v1\n"
        "v_lshlrev_b32_e64 v50, 3, v50\n"
        "v_lshl_add_u32 v49, v49, 4, v50\n"
        "v_lshl_add_u32 v49, v48, 7, v49\n"
        "v_lshrrev_b32_e64 v50, 5, v1\n"
        "v_lshl_add_u32 v49, v50, 11, v49\n"
        "v_add_u32 v2, s70, v49\n"
        "v_xor_b32 v3, 32, v2\n"
        "v_xor_b32_e64 v4, 64, v2\n"
        "v_xor_b32 v5, 0x60, v2\n"
        "s_lshl_b32 s69, s61, 1\n"
        "s_and_b32 s71, s60, 1\n"
        "s_add_u32 s69, s69, s71\n"
        "s_lshl_b32 s69, s69, 2\n"
        "s_add_u32 s69, s69, s68\n"
        "s_lshl_b32 s77, s69, 6\n"
        "s_add_u32 s77, s77, 0x2000\n"
        "s_lshl_b32 s69, s69, 13\n"
        "v_bfe_u32 v49, v1, 4, 1\n"
        "v_lshl_add_u32 v49, v49, 5, v19\n"
        "v_lshlrev_b32 v49, 4, v49\n"
        "v_lshl_add_u32 v49, v50, 10, v49\n"
        "v_add_u32_e64 v6, s69, v49\n"
        "s_lshl_b32 s71, s71, 13\n"
        "s_add_u32 s70, s70, s71\n"
        "v_lshl_add_u32 v13, v17, 3, s77\n"
        "v_xor_b32_e64 v49, v18, v17\n"
        "v_lshl_add_u32 v49, v49, 4, s70\n"
        "v_lshl_add_u32 v9, v19, 8, v49\n"
        "v_xor_b32_e64 v15, 64, v9\n"
        "s_mul_i32 s68, s60, 3072\n"
        "s_add_u32 s68, s68, 0x20000\n"
        "s_xor_b32 s69, s60, 4\n"
        "s_mul_i32 s69, s69, 3072\n"
        "s_add_u32 s69, s69, 0x20000\n"
        "v_lshl_add_u32 v7, v1, 4, s68\n"
        "v_lshl_add_u32 v8, v1, 4, s69\n"
        "s_mov_b32 s74, s78\n"
        "s_add_u32 s75, s78, 0x1000\n"
        "s_lshl_b32 s35, s63, 7\n"
        "s_add_u32 s35, s35, s34\n"
        "s_mul_i32 s3, s63, 0x30000\n"
        "s_add_u32 s63, s63, 1\n"
        "s_and_b32 s63, s63, 7\n"
        "s_add_u32 m0, s33, 0x8000\n"
        "s_nop 0\n"
        "buffer_load_dwordx4 v240, s[20:23], s35 offen lds\n"
        "s_add_u32 m0, s33, 0x8400\n"
        "s_nop 0\n"
        "buffer_load_dwordx4 v241, s[20:23], s35 offen lds\n"
        "s_add_u32 m0, s33, 0xa000\n"
        "s_nop 0\n"
        "buffer_load_dwordx4 v242, s[24:27], s3 offen offset:0 lds\n"
        "s_add_u32 m0, s33, 0xa400\n"
        "s_nop 0\n"
        "buffer_load_dwordx4 v243, s[24:27], s3 offen offset:0 lds\n"
        "s_add_u32 m0, s33, 0xbc00\n"
        "s_nop 0\n"
        "buffer_load_dwordx4 v242, s[24:27], s3 offen offset:1024 lds\n"
        "s_add_u32 m0, s33, 0xc000\n"
        "s_nop 0\n"
        "buffer_load_dwordx4 v243, s[24:27], s3 offen offset:1024 lds\n"
        "s_add_u32 m0, s33, 0xd800\n"
        "s_nop 0\n"
        "buffer_load_dwordx4 v242, s[24:27], s3 offen offset:2048 lds\n"
        "s_add_u32 m0, s33, 0xdc00\n"
        "s_nop 0\n"
        "buffer_load_dwordx4 v243, s[24:27], s3 offen offset:2048 lds\n"
        "s_waitcnt vmcnt(8)\n"
        "s_barrier\n"
        "s_lshl_b32 s35, s63, 7\n"
        "s_add_u32 s35, s35, s34\n"
        "s_mul_i32 s3, s63, 0x30000\n"
        "s_add_u32 s63, s63, 1\n"
        "s_and_b32 s63, s63, 7\n"
        "s_add_u32 m0, s33, 0x10000\n"
        "s_nop 0\n"
        "buffer_load_dwordx4 v240, s[20:23], s35 offen lds\n"
        "s_add_u32 m0, s33, 0x10400\n"
        "s_nop 0\n"
        "buffer_load_dwordx4 v241, s[20:23], s35 offen lds\n"
        "s_add_u32 m0, s33, 0x12000\n"
        "s_nop 0\n"
        "buffer_load_dwordx4 v242, s[24:27], s3 offen offset:0 lds\n"
        "s_add_u32 m0, s33, 0x12400\n"
        "s_nop 0\n"
        "buffer_load_dwordx4 v243, s[24:27], s3 offen offset:0 lds\n"
        "s_add_u32 m0, s33, 0x13c00\n"
        "s_nop 0\n"
        "buffer_load_dwordx4 v242, s[24:27], s3 offen offset:1024 lds\n"
        "s_add_u32 m0, s33, 0x14000\n"
        "s_nop 0\n"
        "buffer_load_dwordx4 v243, s[24:27], s3 offen offset:1024 lds\n"
        "s_add_u32 m0, s33, 0x15800\n"
        "s_nop 0\n"
        "buffer_load_dwordx4 v242, s[24:27], s3 offen offset:2048 lds\n"
        "s_add_u32 m0, s33, 0x15c00\n"
        "s_nop 0\n"
        "buffer_load_dwordx4 v243, s[24:27], s3 offen offset:2048 lds\n"
        "buffer_load_dwordx2 v[252:253], off, s[52:55], 0 sc1\n"
        "s_waitcnt vmcnt(9)\n"
        "s_barrier\n"
        "s_lshl_b32 s35, s63, 7\n"
        "s_add_u32 s35, s35, s34\n"
        "s_mul_i32 s3, s63, 0x30000\n"
        "s_add_u32 s63, s63, 1\n"
        "s_and_b32 s63, s63, 7\n"
        "s_add_u32 m0, s33, 0x18000\n"
        "s_nop 0\n"
        "buffer_load_dwordx4 v240, s[20:23], s35 offen lds\n"
        "s_add_u32 m0, s33, 0x18400\n"
        "s_nop 0\n"
        "buffer_load_dwordx4 v241, s[20:23], s35 offen lds\n"
        "s_add_u32 m0, s33, 0x1a000\n"
        "s_nop 0\n"
        "buffer_load_dwordx4 v242, s[24:27], s3 offen offset:0 lds\n"
        "s_add_u32 m0, s33, 0x1a400\n"
        "s_nop 0\n"
        "buffer_load_dwordx4 v243, s[24:27], s3 offen offset:0 lds\n"
        "s_add_u32 m0, s33, 0x1bc00\n"
        "s_nop 0\n"
        "buffer_load_dwordx4 v242, s[24:27], s3 offen offset:1024 lds\n"
        "s_add_u32 m0, s33, 0x1c000\n"
        "s_nop 0\n"
        "buffer_load_dwordx4 v243, s[24:27], s3 offen offset:1024 lds\n"
        "s_add_u32 m0, s33, 0x1d800\n"
        "s_nop 0\n"
        "buffer_load_dwordx4 v242, s[24:27], s3 offen offset:2048 lds\n"
        "s_add_u32 m0, s33, 0x1dc00\n"
        "s_nop 0\n"
        "buffer_load_dwordx4 v243, s[24:27], s3 offen offset:2048 lds\n"
        "s_waitcnt vmcnt(9)\n"
        "s_barrier\n"
        "s_lshl_b32 s35, s63, 7\n"
        "s_add_u32 s35, s35, s34\n"
        "s_mul_i32 s3, s63, 0x30000\n"
        "s_add_u32 s63, s63, 1\n"
        "s_and_b32 s63, s63, 7\n"
        "s_add_u32 m0, s33, 0x0\n"
        "s_nop 0\n"
        "buffer_load_dwordx4 v240, s[20:23], s35 offen lds\n"
        "s_add_u32 m0, s33, 0x400\n"
        "s_nop 0\n"
        "buffer_load_dwordx4 v241, s[20:23], s35 offen lds\n"
        "s_add_u32 m0, s33, 0x2000\n"
        "s_nop 0\n"
        "buffer_load_dwordx4 v242, s[24:27], s3 offen offset:0 lds\n"
        "s_add_u32 m0, s33, 0x2400\n"
        "s_nop 0\n"
        "buffer_load_dwordx4 v243, s[24:27], s3 offen offset:0 lds\n"
        "s_add_u32 m0, s33, 0x3c00\n"
        "s_nop 0\n"
        "buffer_load_dwordx4 v242, s[24:27], s3 offen offset:1024 lds\n"
        "s_add_u32 m0, s33, 0x4000\n"
        "s_nop 0\n"
        "buffer_load_dwordx4 v243, s[24:27], s3 offen offset:1024 lds\n"
        "s_add_u32 m0, s33, 0x5800\n"
        "s_nop 0\n"
        "buffer_load_dwordx4 v242, s[24:27], s3 offen offset:2048 lds\n"
        "s_add_u32 m0, s33, 0x5c00\n"
        "s_nop 0\n"
        "buffer_load_dwordx4 v243, s[24:27], s3 offen offset:2048 lds\n"
        "s_waitcnt vmcnt(8)\n"
        "s_barrier\n"
        "v_readfirstlane_b32 s68, v252\n"
        "v_readfirstlane_b32 s69, v253\n"
        "s_mul_i32 s68, s68, 0x9e3779b1\n"
        "s_add_u32 s64, s68, 0x7f4a7c15\n"
        "s_mul_i32 s69, s69, 0x85ebca6d\n"
        "s_add_u32 s65, s69, 0xc2b2ae3d\n"
        "s_lshl_b32 s35, s63, 7\n"
        "s_add_u32 s35, s35, s34\n"
        "s_mul_i32 s3, s63, 0x30000\n"
        "s_add_u32 s63, s63, 1\n"
        "s_and_b32 s63, s63, 7\n"
        "s_add_u32 m0, s33, 0x8000\n"
        "s_nop 0\n"
        "buffer_load_dwordx4 v240, s[20:23], s35 offen lds\n"
        "s_add_u32 m0, s33, 0x8400\n"
        "s_nop 0\n"
        "buffer_load_dwordx4 v241, s[20:23], s35 offen lds\n"
        "s_add_u32 m0, s33, 0xa000\n"
        "s_nop 0\n"
        "buffer_load_dwordx4 v242, s[24:27], s3 offen offset:0 lds\n"
        "s_add_u32 m0, s33, 0xa400\n"
        "s_nop 0\n"
        "buffer_load_dwordx4 v243, s[24:27], s3 offen offset:0 lds\n"
        "s_add_u32 m0, s33, 0xbc00\n"
        "s_nop 0\n"
        "buffer_load_dwordx4 v242, s[24:27], s3 offen offset:1024 lds\n"
        "s_add_u32 m0, s33, 0xc000\n"
        "s_nop 0\n"
        "buffer_load_dwordx4 v243, s[24:27], s3 offen offset:1024 lds\n"
        "s_add_u32 m0, s33, 0xd800\n"
        "s_nop 0\n"
        "buffer_load_dwordx4 v242, s[24:27], s3 offen offset:2048 lds\n"
        "s_add_u32 m0, s33, 0xdc00\n"
        "s_nop 0\n"
        "buffer_load_dwordx4 v243, s[24:27], s3 offen offset:2048 lds\n"
        "s_waitcnt vmcnt(8)\n"
        "s_barrier\n"
        "s_lshl_b32 s35, s63, 7\n"
        "s_add_u32 s35, s35, s34\n"
        "s_mul_i32 s3, s63, 0x30000\n"
        "s_add_u32 s63, s63, 1\n"
        "s_and_b32 s63, s63, 7\n"
        "s_add_u32 m0, s33, 0x10000\n"
        "s_nop 0\n"
        "buffer_load_dwordx4 v240, s[20:23], s35 offen lds\n"
        "s_add_u32 m0, s33, 0x10400\n"
        "s_nop 0\n"
        "buffer_load_dwordx4 v241, s[20:23], s35 offen lds\n"
        "s_add_u32 m0, s33, 0x12000\n"
        "s_nop 0\n"
        "buffer_load_dwordx4 v242, s[24:27], s3 offen offset:0 lds\n"
        "s_add_u32 m0, s33, 0x12400\n"
        "s_nop 0\n"
        "buffer_load_dwordx4 v243, s[24:27], s3 offen offset:0 lds\n"
        "s_add_u32 m0, s33, 0x13c00\n"
        "s_nop 0\n"
        "buffer_load_dwordx4 v242, s[24:27], s3 offen offset:1024 lds\n"
        "s_add_u32 m0, s33, 0x14000\n"
        "s_nop 0\n"
        "buffer_load_dwordx4 v243, s[24:27], s3 offen offset:1024 lds\n"
        "s_add_u32 m0, s33, 0x15800\n"
        "s_nop 0\n"
        "buffer_load_dwordx4 v242, s[24:27], s3 offen offset:2048 lds\n"
        "s_add_u32 m0, s33, 0x15c00\n"
        "s_nop 0\n"
        "buffer_load_dwordx4 v243, s[24:27], s3 offen offset:2048 lds\n"
        "s_waitcnt vmcnt(8)\n"
        "s_barrier\n"
        "s_lshl_b32 s35, s63, 7\n"
        "s_add_u32 s35, s35, s34\n"
        "s_mul_i32 s3, s63, 0x30000\n"
        "s_add_u32 s63, s63, 1\n"
        "s_and_b32 s63, s63, 7\n"
        "s_add_u32 m0, s33, 0x18000\n"
        "s_nop 0\n"
        "buffer_load_dwordx4 v240, s[20:23], s35 offen lds\n"
        "s_add_u32 m0, s33, 0x18400\n"
        "s_nop 0\n"
        "buffer_load_dwordx4 v241, s[20:23], s35 offen lds\n"
        "s_add_u32 m0, s33, 0x1a000\n"
        "s_nop 0\n"
        "buffer_load_dwordx4 v242, s[24:27], s3 offen offset:0 lds\n"
        "s_add_u32 m0, s33, 0x1a400\n"
        "s_nop 0\n"
        "buffer_load_dwordx4 v243, s[24:27], s3 offen offset:0 lds\n"
        "s_add_u32 m0, s33, 0x1bc00\n"
        "s_nop 0\n"
        "buffer_load_dwordx4 v242, s[24:27], s3 offen offset:1024 lds\n"
        "s_add_u32 m0, s33, 0x1c000\n"
        "s_nop 0\n"
        "buffer_load_dwordx4 v243, s[24:27], s3 offen offset:1024 lds\n"
        "s_add_u32 m0, s33, 0x1d800\n"
        "s_nop 0\n"
        "buffer_load_dwordx4 v242, s[24:27], s3 offen offset:2048 lds\n"
        "s_add_u32 m0, s33, 0x1dc00\n"
        "s_nop 0\n"
        "buffer_load_dwordx4 v243, s[24:27], s3 offen offset:2048 lds\n"
        "s_waitcnt vmcnt(8)\n"
        "s_barrier\n"
        "v_xor_b32 v242, 128, v242\n"
        "v_xor_b32 v243, 128, v243\n"
        "s_lshl_b32 s35, s63, 7\n"
        "s_add_u32 s35, s35, s34\n"
        "s_mul_i32 s3, s63, 0x30000\n"
        "s_add_u32 s63, s63, 1\n"
        "s_and_b32 s63, s63, 7\n"
        "s_add_u32 m0, s33, 0x0\n"
        "s_nop 0\n"
        "buffer_load_dwordx4 v240, s[28:31], s35 offen lds\n"
        "s_add_u32 m0, s33, 0x400\n"
        "s_nop 0\n"
        "buffer_load_dwordx4 v241, s[28:31], s35 offen lds\n"
        "s_add_u32 m0, s33, 0x2000\n"
        "s_nop 0\n"
        "buffer_load_dwordx4 v242, s[36:39], s3 offen offset:0 lds\n"
        "s_add_u32 m0, s33, 0x2400\n"
        "s_nop 0\n"
        "buffer_load_dwordx4 v243, s[36:39], s3 offen offset:0 lds\n"
        "s_add_u32 m0, s33, 0x3c00\n"
        "s_nop 0\n"
        "buffer_load_dwordx4 v242, s[36:39], s3 offen offset:1024 lds\n"
        "s_add_u32 m0, s33, 0x4000\n"
        "s_nop 0\n"
        "buffer_load_dwordx4 v243, s[36:39], s3 offen offset:1024 lds\n"
        "s_add_u32 m0, s33, 0x5800\n"
        "s_nop 0\n"
        "buffer_load_dwordx4 v242, s[36:39], s3 offen offset:2048 lds\n"
        "s_add_u32 m0, s33, 0x5c00\n"
        "s_nop 0\n"
        "buffer_load_dwordx4 v243, s[36:39], s3 offen offset:2048 lds\n"
        "s_waitcnt vmcnt(8)\n"
        "s_barrier\n"
        "s_lshl_b32 s35, s63, 7\n"
        "s_add_u32 s35, s35, s34\n"
        "s_mul_i32 s3, s63, 0x30000\n"
        "s_add_u32 s63, s63, 1\n"
        "s_and_b32 s63, s63, 7\n"
        "s_add_u32 m0, s33, 0x8000\n"
        "s_nop 0\n"
        "buffer_load_dwordx4 v240, s[28:31], s35 offen lds\n"
        "s_add_u32 m0, s33, 0x8400\n"
        "s_nop 0\n"
        "buffer_load_dwordx4 v241, s[28:31], s35 offen lds\n"
        "s_add_u32 m0, s33, 0xa000\n"
        "s_nop 0\n"
        "buffer_load_dwordx4 v242, s[36:39], s3 offen offset:0 lds\n"
        "s_add_u32 m0, s33, 0xa400\n"
        "s_nop 0\n"
        "buffer_load_dwordx4 v243, s[36:39], s3 offen offset:0 lds\n"
        "s_add_u32 m0, s33, 0xbc00\n"
        "s_nop 0\n"
        "buffer_load_dwordx4 v242, s[36:39], s3 offen offset:1024 lds\n"
        "s_add_u32 m0, s33, 0xc000\n"
        "s_nop 0\n"
        "buffer_load_dwordx4 v243, s[36:39], s3 offen offset:1024 lds\n"
        "s_add_u32 m0, s33, 0xd800\n"
        "s_nop 0\n"
        "buffer_load_dwordx4 v242, s[36:39], s3 offen offset:2048 lds\n"
        "s_add_u32 m0, s33, 0xdc00\n"
        "s_nop 0\n"
        "buffer_load_dwordx4 v243, s[36:39], s3 offen offset:2048 lds\n"
        "s_waitcnt vmcnt(8)\n"
        "s_barrier\n"
        "s_lshl_b32 s35, s63, 7\n"
        "s_add_u32 s35, s35, s34\n"
        "s_mul_i32 s3, s63, 0x30000\n"
        "s_add_u32 s63, s63, 1\n"
        "s_and_b32 s63, s63, 7\n"
        "s_add_u32 m0, s33, 0x10000\n"
        "s_nop 0\n"
        "buffer_load_dwordx4 v240, s[28:31], s35 offen lds\n"
        "s_add_u32 m0, s33, 0x10400\n"
        "s_nop 0\n"
        "buffer_load_dwordx4 v241, s[28:31], s35 offen lds\n"
        "s_add_u32 m0, s33, 0x12000\n"
        "s_nop 0\n"
        "buffer_load_dwordx4 v242, s[36:39], s3 offen offset:0 lds\n"
        "s_add_u32 m0, s33, 0x12400\n"
        "s_nop 0\n"
        "buffer_load_dwordx4 v243, s[36:39], s3 offen offset:0 lds\n"
        "s_add_u32 m0, s33, 0x13c00\n"
        "s_nop 0\n"
        "buffer_load_dwordx4 v242, s[36:39], s3 offen offset:1024 lds\n"
        "s_add_u32 m0, s33, 0x14000\n"
        "s_nop 0\n"
        "buffer_load_dwordx4 v243, s[36:39], s3 offen offset:1024 lds\n"
        "s_add_u32 m0, s33, 0x15800\n"
        "s_nop 0\n"
        "buffer_load_dwordx4 v242, s[36:39], s3 offen offset:2048 lds\n"
        "s_add_u32 m0, s33, 0x15c00\n"
        "s_nop 0\n"
        "buffer_load_dwordx4 v243, s[36:39], s3 offen offset:2048 lds\n"
        "s_waitcnt vmcnt(8)\n"
        "s_barrier\n"
        "s_lshl_b32 s35, s63, 7\n"
        "s_add_u32 s35, s35, s34\n"
        "s_mul_i32 s3, s63, 0x30000\n"
        "s_add_u32 s63, s63, 1\n"
        "s_and_b32 s63, s63, 7\n"
        "s_add_u32 m0, s33, 0x18000\n"
        "s_nop 0\n"
        "buffer_load_dwordx4 v240, s[28:31], s35 offen lds\n"
        "s_add_u32 m0, s33, 0x18400\n"
        "s_nop 0\n"
        "buffer_load_dwordx4 v241, s[28:31], s35 offen lds\n"
        "s_add_u32 m0, s33, 0x1a000\n"
        "s_nop 0\n"
        "buffer_load_dwordx4 v242, s[36:39], s3 offen offset:0 lds\n"
        "s_add_u32 m0, s33, 0x1a400\n"
        "s_nop 0\n"
        "buffer_load_dwordx4 v243, s[36:39], s3 offen offset:0 lds\n"
        "s_add_u32 m0, s33, 0x1bc00\n"
        "s_nop 0\n"
        "buffer_load_dwordx4 v242, s[36:39], s3 offen offset:1024 lds\n"
        "s_add_u32 m0, s33, 0x1c000\n"
        "s_nop 0\n"
        "buffer_load_dwordx4 v243, s[36:39], s3 offen offset:1024 lds\n"
        "s_add_u32 m0, s33, 0x1d800\n"
        "s_nop 0\n"
        "buffer_load_dwordx4 v242, s[36:39], s3 offen offset:2048 lds\n"
        "s_add_u32 m0, s33, 0x1dc00\n"
        "s_nop 0\n"
        "buffer_load_dwordx4 v243, s[36:39], s3 offen offset:2048 lds\n"
        "ds_read_b128 v[176:179], v227\n"
        "ds_read_b128 v[180:183], v226\n"
        "s_waitcnt vmcnt(8)\n"
        "s_barrier\n"
        "s_cmp_lg_u32 s60, 4\n"
        "s_cbranch_scc1 L_noflag_%=\n"
        "v_mov_b32_e64 v228, s64\n"
        "v_mov_b32 v229, s65\n"
        "s_mov_b64 exec, 1\n"
        "buffer_store_dwordx2 v[228:229], off, s[52:55], s7 sc1\n"
        "s_mov_b64 exec, -1\n"
        "L_noflag_%=:\n"
        "s_lshl_b32 s35, s63, 7\n"
        "s_add_u32 s35, s35, s34\n"
        "s_mul_i32 s3, s63, 0x30000\n"
        "s_add_u32 s63, s63, 1\n"
        "s_and_b32 s63, s63, 7\n"
        "s_add_u32 m0, s33, 0x0\n"
        "s_nop 0\n"
        "buffer_load_dwordx4 v240, s[28:31], s35 offen lds\n"
        "s_add_u32 m0, s33, 0x400\n"
        "s_nop 0\n"
        "buffer_load_dwordx4 v241, s[28:31], s35 offen lds\n"
        "s_add_u32 m0, s33, 0x2000\n"
        "s_nop 0\n"
        "buffer_load_dwordx4 v242, s[36:39], s3 offen offset:0 lds\n"
        "s_add_u32 m0, s33, 0x2400\n"
        "s_nop 0\n"
        "buffer_load_dwordx4 v243, s[36:39], s3 offen offset:0 lds\n"
        "s_add_u32 m0, s33, 0x3c00\n"
        "s_nop 0\n"
        "buffer_load_dwordx4 v242, s[36:39], s3 offen offset:1024 lds\n"
        "s_add_u32 m0, s33, 0x4000\n"
        "s_nop 0\n"
        "buffer_load_dwordx4 v243, s[36:39], s3 offen offset:1024 lds\n"
        "s_add_u32 m0, s33, 0x5800\n"
        "s_nop 0\n"
        "buffer_load_dwordx4 v242, s[36:39], s3 offen offset:2048 lds\n"
        "s_add_u32 m0, s33, 0x5c00\n"
        "s_nop 0\n"
        "buffer_load_dwordx4 v243, s[36:39], s3 offen offset:2048 lds\n"
        "s_waitcnt vmcnt(8)\n"
        "s_barrier\n"
        "s_lshl_b32 s35, s63, 7\n"
        "s_add_u32 s35, s35, s34\n"
        "s_mul_i32 s3, s63, 0x30000\n"
        "s_add_u32 s63, s63, 1\n"
        "s_and_b32 s63, s63, 7\n"
        "s_add_u32 m0, s33, 0x8000\n"
        "s_nop 0\n"
        "buffer_load_dwordx4 v240, s[28:31], s35 offen lds\n"
        "s_add_u32 m0, s33, 0x8400\n"
        "s_nop 0\n"
        "buffer_load_dwordx4 v241, s[28:31], s35 offen lds\n"
        "s_add_u32 m0, s33, 0xa000\n"
        "s_nop 0\n"
        "buffer_load_dwordx4 v242, s[36:39], s3 offen offset:0 lds\n"
        "s_add_u32 m0, s33, 0xa400\n"
        "s_nop 0\n"
        "buffer_load_dwordx4 v243, s[36:39], s3 offen offset:0 lds\n"
        "s_add_u32 m0, s33, 0xbc00\n"
        "s_nop 0\n"
        "buffer_load_dwordx4 v242, s[36:39], s3 offen offset:1024 lds\n"
        "s_add_u32 m0, s33, 0xc000\n"
        "s_nop 0\n"
        "buffer_load_dwordx4 v243, s[36:39], s3 offen offset:1024 lds\n"
        "s_add_u32 m0, s33, 0xd800\n"
        "s_nop 0\n"
        "buffer_load_dwordx4 v242, s[36:39], s3 offen offset:2048 lds\n"
        "s_add_u32 m0, s33, 0xdc00\n"
        "s_nop 0\n"
        "buffer_load_dwordx4 v243, s[36:39], s3 offen offset:2048 lds\n"
        "s_waitcnt vmcnt(8)\n"
        "s_barrier\n"
        "s_lshl_b32 s35, s63, 7\n"
        "s_add_u32 s35, s35, s34\n"
        "s_mul_i32 s3, s63, 0x30000\n"
        "s_add_u32 s63, s63, 1\n"
        "s_and_b32 s63, s63, 7\n"
        "s_add_u32 m0, s33, 0x10000\n"
        "s_nop 0\n"
        "buffer_load_dwordx4 v240, s[28:31], s35 offen lds\n"
        "s_add_u32 m0, s33, 0x10400\n"
        "s_nop 0\n"
        "buffer_load_dwordx4 v241, s[28:31], s35 offen lds\n"
        "s_add_u32 m0, s33, 0x12000\n"
        "s_nop 0\n"
        "buffer_load_dwordx4 v242, s[36:39], s3 offen offset:0 lds\n"
        "s_add_u32 m0, s33, 0x12400\n"
        "s_nop 0\n"
        "buffer_load_dwordx4 v243, s[36:39], s3 offen offset:0 lds\n"
        "s_add_u32 m0, s33, 0x13c00\n"
        "s_nop 0\n"
        "buffer_load_dwordx4 v242, s[36:39], s3 offen offset:1024 lds\n"
        "s_add_u32 m0, s33, 0x14000\n"
        "s_nop 0\n"
        "buffer_load_dwordx4 v243, s[36:39], s3 offen offset:1024 lds\n"
        "s_add_u32 m0, s33, 0x15800\n"
        "s_nop 0\n"
        "buffer_load_dwordx4 v242, s[36:39], s3 offen offset:2048 lds\n"
        "s_add_u32 m0, s33, 0x15c00\n"
        "s_nop 0\n"
        "buffer_load_dwordx4 v243, s[36:39], s3 offen offset:2048 lds\n"
        "s_waitcnt vmcnt(8)\n"
        "s_barrier\n"
        "s_lshl_b32 s35, s63, 7\n"
        "s_add_u32 s35, s35, s34\n"
        "s_mul_i32 s3, s63, 0x30000\n"
        "s_add_u32 s63, s63, 1\n"
        "s_and_b32 s63, s63, 7\n"
        "s_add_u32 m0, s33, 0x18000\n"
        "s_nop 0\n"
        "buffer_load_dwordx4 v240, s[28:31], s35 offen lds\n"
        "s_add_u32 m0, s33, 0x18400\n"
        "s_nop 0\n"
        "buffer_load_dwordx4 v241, s[28:31], s35 offen lds\n"
        "s_add_u32 m0, s33, 0x1a000\n"
        "s_nop 0\n"
        "buffer_load_dwordx4 v242, s[36:39], s3 offen offset:0 lds\n"
        "s_add_u32 m0, s33, 0x1a400\n"
        "s_nop 0\n"
        "buffer_load_dwordx4 v243, s[36:39], s3 offen offset:0 lds\n"
        "s_add_u32 m0, s33, 0x1bc00\n"
        "s_nop 0\n"
        "buffer_load_dwordx4 v242, s[36:39], s3 offen offset:1024 lds\n"
        "s_add_u32 m0, s33, 0x1c000\n"
        "s_nop 0\n"
        "buffer_load_dwordx4 v243, s[36:39], s3 offen offset:1024 lds\n"
        "s_add_u32 m0, s33, 0x1d800\n"
        "s_nop 0\n"
        "buffer_load_dwordx4 v242, s[36:39], s3 offen offset:2048 lds\n"
        "s_add_u32 m0, s33, 0x1dc00\n"
        "s_nop 0\n"
        "buffer_load_dwordx4 v243, s[36:39], s3 offen offset:2048 lds\n"
        "s_waitcnt vmcnt(8)\n"
        "s_barrier\n"
        "buffer_load_dwordx2 v[252:253], v14, s[52:55], 0 offen sc1\n"
        "s_waitcnt vmcnt(1)\n"
        "s_barrier\n"
        "s_waitcnt vmcnt(0)\n"
        "v_cmp_ne_u64 s[68:69], v[252:253], s[64:65]\n"
        "s_nop 0\n"
        "s_cmp_lg_u64 s[68:69], 0\n"
        "s_cbranch_scc1 L_fl_r_%=\n"
        "L_fl_d_%=:\n"
        "buffer_load_dwordx4 v[128:131], v11, s[52:55], s74 offen offset:0 sc1\n"
        "s_mov_b64 exec, s[66:67]\n"
        "buffer_load_dwordx4 v[132:135], v12, s[52:55], s74 offen offset:0 sc1\n"
        "s_mov_b64 exec, -1\n"
        "s_mov_b64 exec, s[66:67]\n"
        "buffer_load_dwordx4 v[136:139], v12, s[52:55], s74 offen offset:512 sc1\n"
        "s_mov_b64 exec, -1\n"
        "buffer_load_dwordx4 v[140:143], v11, s[52:55], s74 offen offset:2048 sc1\n"
        "s_mov_b64 exec, s[66:67]\n"
        "buffer_load_dwordx4 v[144:147], v12, s[52:55], s74 offen offset:2048 sc1\n"
        "s_mov_b64 exec, -1\n"
        "s_mov_b64 exec, s[66:67]\n"
        "buffer_load_dwordx4 v[148:151], v12, s[52:55], s74 offen offset:2560 sc1\n"
        "s_mov_b64 exec, -1\n"
        "buffer_load_dwordx4 v[152:155], v11, s[52:55], s75 offen offset:0 sc1\n"
        "s_mov_b64 exec, s[66:67]\n"
        "buffer_load_dwordx4 v[156:159], v12, s[52:55], s75 offen offset:0 sc1\n"
        "s_mov_b64 exec, -1\n"
        "s_mov_b64 exec, s[66:67]\n"
        "buffer_load_dwordx4 v[160:163], v12, s[52:55], s75 offen offset:512 sc1\n"
        "s_mov_b64 exec, -1\n"
        "buffer_load_dwordx4 v[164:167], v11, s[52:55], s75 offen offset:2048 sc1\n"
        "s_mov_b64 exec, s[66:67]\n"
        "buffer_load_dwordx4 v[168:171], v12, s[52:55], s75 offen offset:2048 sc1\n"
        "s_mov_b64 exec, -1\n"
        "s_waitcnt lgkmcnt(0)\n"
        "s_barrier\n"
        "s_mov_b64 vcc, -1\n"
        "s_branch L_ATT_%=\n"
        "L_KVROLE_%=:\n"
        "s_waitcnt lgkmcnt(0)\n"
        "s_mov_b32 s52, s18\n"
        "s_and_b32 s53, s19, 0xffff\n"
        "s_mov_b32 s54, 0x1000000\n"
        "s_mov_b32 s55, 0x00020000\n"
        "s_mov_b32 s40, s8\n"
        "s_and_b32 s41, s9, 0xffff\n"
        "s_mov_b32 s42, 0x80000\n"
        "s_mov_b32 s43, 0x00020000\n"
        "s_mov_b32 s44, s14\n"
        "s_and_b32 s45, s15, 0xffff\n"
        "s_mov_b32 s46, 0x80000\n"
        "s_mov_b32 s47, 0x00020000\n"
        "s_mov_b32 s48, s16\n"
        "s_and_b32 s49, s17, 0xffff\n"
        "s_mov_b32 s50, 0x400000\n"
        "s_mov_b32 s51, 0x00020000\n"
        "v_bfe_u32 v20, v1, 1, 3\n"
        "v_xor_b32 v20, v20, v18\n"
        "v_lshlrev_b32_e64 v20, 4, v20\n"
        "v_lshl_add_u32 v236, v19, 7, v20\n"
        "v_xor_b32_e64 v237, 64, v236\n"
        "v_bfe_u32 v20, v1, 1, 1\n"
        "v_bfe_u32 v21, v1, 3, 2\n"
        "v_lshl_or_b32 v20, v21, 1, v20\n"
        "v_and_b32 v21, 1, v1\n"
        "v_lshlrev_b32 v21, 3, v21\n"
        "v_lshl_add_u32 v20, v20, 4, v21\n"
        "v_bfe_u32 v21, v1, 2, 2\n"
        "v_lshl_add_u32 v20, v21, 7, v20\n"
        "v_lshl_add_u32 v20, v18, 10, v20\n"
        "v_add_u32 v238, 0x2000, v20\n"
        "buffer_load_dwordx2 v[252:253], off, s[52:55], 0 sc1\n"
        "s_lshl_b32 s68, s60, 5\n"
        "v_xor_b32 v238, s68, v238\n"
        "s_mov_b32 s66, 0xffff\n"
        "s_mov_b32 s67, 0xffff\n"
        "s_mov_b32 s72, 0x40000\n"
        "s_and_b32 s68, s60, 3\n"
        "s_lshl_b32 s69, s62, 2\n"
        "s_add_u32 s70, s68, s69\n"
        "s_lshl_b32 s68, s70, 17\n"
        "s_lshr_b32 s71, s60, 2\n"
        "s_lshl_b32 s69, s71, 16\n"
        "s_add_u32 s68, s68, s69\n"
        "s_add_u32 s68, s68, 0x100000\n"
        "v_lshl_add_u32 v11, v1, 4, s68\n"
        "v_lshlrev_b32 v20, 4, v19\n"
        "v_lshrrev_b32_e64 v21, 5, v1\n"
        "v_lshl_add_u32 v20, v21, 8, v20\n"
        "s_add_u32 s68, s68, 0x400\n"
        "v_add_u32 v12, s68, v20\n"
        "s_and_b32 s68, s61, 0\n"
        "s_lshl_b32 s78, s68, 13\n"
        "s_lshl_b32 s68, s61, 1\n"
        "s_add_u32 s68, s68, s71\n"
        "s_lshl_b32 s68, s68, 5\n"
        "s_add_u32 s68, s68, s70\n"
        "s_lshl_b32 s68, s68, 10\n"
        "v_lshl_add_u32 v255, v1, 4, s68\n"
        "s_and_b32 s68, s60, 3\n"
        "s_lshl_b32 s68, s68, 13\n"
        "s_xor_b32 s69, s73, 384\n"
        "s_add_u32 s68, s68, s69\n"
        "v_and_b32 v21, 6, v16\n"
        "v_xor_b32_e64 v21, v21, v17\n"
        "v_lshl_add_u32 v21, v21, 4, s68\n"
        "v_lshl_add_u32 v254, v16, 10, v21\n"
        "s_and_b32 s68, s60, 3\n"
        "s_lshl_b32 s68, s68, 11\n"
        "s_add_u32 s68, s68, 0x20000\n"
        "s_lshr_b32 s69, s60, 2\n"
        "s_lshl_b32 s69, s69, 10\n"
        "s_add_u32 s68, s68, s69\n"
        "v_lshl_add_u32 v226, v1, 4, s68\n"
        "v_xor_b32 v227, 0x400, v226\n"
        "v_and_b32 v21, 27, v1\n"
        "v_cmp_eq_u32 vcc, 16, v21\n"
        "v_mov_b32 v22, 0x3f803f80\n"
        "v_mov_b32 v10, 0xdddddddd\n"
        "v_cndmask_b32 v21, 0, v22, vcc\n"
        "v_mov_b32 v132, v21\n"
        "v_mov_b32 v133, v21\n"
        "v_mov_b32 v134, v21\n"
        "v_mov_b32 v135, v21\n"
        "v_mov_b32 v136, v21\n"
        "v_mov_b32 v137, v21\n"
        "v_mov_b32 v138, v21\n"
        "v_mov_b32 v139, v21\n"
        "v_mov_b32 v144, v21\n"
        "v_mov_b32 v145, v21\n"
        "v_mov_b32 v146, v21\n"
        "v_mov_b32 v147, v21\n"
        "v_mov_b32 v148, v21\n"
        "v_mov_b32 v149, v21\n"
        "v_mov_b32 v150, v21\n"
        "v_mov_b32 v151, v21\n"
        "v_mov_b32 v156, v21\n"
        "v_mov_b32 v157, v21\n"
        "v_mov_b32 v158, v21\n"
        "v_mov_b32 v159, v21\n"
        "v_mov_b32 v160, v21\n"
        "v_mov_b32 v161, v21\n"
        "v_mov_b32 v162, v21\n"
        "v_mov_b32 v163, v21\n"
        "v_mov_b32 v168, v21\n"
        "v_mov_b32 v169, v21\n"
        "v_mov_b32 v170, v21\n"
        "v_mov_b32 v171, v21\n"
        "v_mov_b32 v172, v21\n"
        "v_mov_b32 v173, v21\n"
        "v_mov_b32_e64 v174, v21\n"
        "v_mov_b32 v175, v21\n"
        "s_lshl_b32 s68, s62, 2\n"
        "s_add_u32 s68, s68, s60\n"
        "s_lshl_b32 s68, s68, 5\n"
        "s_add_u32 s68, s68, s61\n"
        "s_lshl_b32 s68, s68, 12\n"
        "s_add_u32 s4, s68, 0x100000\n"
        "v_lshlrev_b32 v240, 4, v1\n"
        "v_and_b32 v21, 1, v18\n"
        "v_lshl_add_u32 v241, v21, 9, v20\n"
        "s_lshl_b32 s69, s62, 5\n"
        "s_add_u32 s69, s69, s61\n"
        "s_mul_i32 s69, s69, 4352\n"
        "s_add_u32 s69, s69, 0x900000\n"
        "s_lshl_b32 s70, s60, 10\n"
        "s_add_u32 s6, s69, s70\n"
        "s_lshl_b32 s70, s60, 6\n"
        "s_add_u32 s69, s69, 0x1000\n"
        "s_add_u32 s7, s69, s70\n"
        "v_lshlrev_b32_e64 v21, 4, v21\n"
        "v_lshl_add_u32 v242, v20, 1, v21\n"
        "v_lshlrev_b32_e64 v243, 4, v18\n"
        "v_mov_b32 v232, 0x3f803f80\n"
        "v_mov_b32 v233, 0x3f803f80\n"
        "v_mov_b32 v234, 0x3f803f80\n"
        "v_mov_b32 v235, 0x3f803f80\n"
        "v_mov_b32 v184, 0\n"
        "v_mov_b32 v185, 0\n"
        "v_mov_b32 v186, 0\n"
        "v_mov_b32 v187, 0\n"
        "v_mov_b32 v188, 0\n"
        "v_mov_b32 v189, 0\n"
        "v_mov_b32 v190, 0\n"
        "v_mov_b32 v191, 0\n"
        "v_mov_b32 v192, 0\n"
        "v_mov_b32 v193, 0\n"
        "v_mov_b32 v194, 0\n"
        "v_mov_b32 v195, 0\n"
        "v_mov_b32 v196, 0\n"
        "v_mov_b32 v197, 0\n"
        "v_mov_b32 v198, 0\n"
        "v_mov_b32 v199, 0\n"
        "v_mov_b32 v200, 0\n"
        "v_mov_b32 v201, 0\n"
        "v_mov_b32 v202, 0\n"
        "v_mov_b32 v203, 0\n"
        "v_mov_b32 v204, 0\n"
        "v_mov_b32 v205, 0\n"
        "v_mov_b32 v206, 0\n"
        "v_mov_b32 v207, 0\n"
        "v_mov_b32 v208, 0\n"
        "v_mov_b32 v209, 0\n"
        "v_mov_b32 v210, 0\n"
        "v_mov_b32 v211, 0\n"
        "v_mov_b32 v212, 0\n"
        "v_mov_b32 v213, 0\n"
        "v_mov_b32 v214, 0\n"
        "v_mov_b32 v215, 0\n"
        "s_mov_b32 s14, 0x701000\n"
        "s_bitcmp1_b32 s62, 0\n"
        "s_cselect_b32 s14, 0x6ff000, s14\n"
        "s_lshl_b32 s18, s62, 0\n"
        "s_lshr_b32 s68, s60, 2\n"
        "s_add_u32 s68, s68, s18\n"
        "s_and_b32 s69, s68, 3\n"
        "s_lshl_b32 s12, s69, 15\n"
        "s_xor_b32 s13, s12, 0x10000\n"
        "s_and_b32 s76, s61, 7\n"
        "s_lshr_b32 s68, s60, 1\n"
        "s_lshl_b32 s70, s68, 14\n"
        "v_bfe_u32 v48, v1, 2, 3\n"
        "v_and_b32 v49, 6, v48\n"
        "v_bfe_u32 v50, v1, 1, 1\n"
        "v_or_b32 v49, v49, v50\n"
        "v_and_b32 v50, 1, v1\n"
        "v_lshlrev_b32_e64 v50, 3, v50\n"
        "v_lshl_add_u32 v49, v49, 4, v50\n"
        "v_lshl_add_u32 v49, v48, 7, v49\n"
        "v_lshrrev_b32_e64 v50, 5, v1\n"
        "v_lshl_add_u32 v49, v50, 11, v49\n"
        "v_add_u32 v2, s70, v49\n"
        "v_xor_b32 v3, 32, v2\n"
        "v_xor_b32_e64 v4, 64, v2\n"
        "v_xor_b32 v5, 0x60, v2\n"
        "s_lshl_b32 s69, s61, 1\n"
        "s_and_b32 s71, s60, 1\n"
        "s_add_u32 s69, s69, s71\n"
        "s_lshl_b32 s69, s69, 2\n"
        "s_add_u32 s69, s69, s68\n"
        "s_lshl_b32 s77, s69, 6\n"
        "s_add_u32 s77, s77, 0x2000\n"
        "s_lshl_b32 s69, s69, 13\n"
        "v_bfe_u32 v49, v1, 4, 1\n"
        "v_lshl_add_u32 v49, v49, 5, v19\n"
        "v_lshlrev_b32 v49, 4, v49\n"
        "v_lshl_add_u32 v49, v50, 10, v49\n"
        "v_add_u32_e64 v6, s69, v49\n"
        "s_lshl_b32 s71, s71, 13\n"
        "s_add_u32 s70, s70, s71\n"
        "v_lshl_add_u32 v13, v17, 3, s77\n"
        "v_xor_b32_e64 v49, v18, v17\n"
        "v_lshl_add_u32 v49, v49, 4, s70\n"
        "v_lshl_add_u32 v9, v19, 8, v49\n"
        "v_xor_b32_e64 v15, 64, v9\n"
        "s_mul_i32 s68, s60, 3072\n"
        "s_add_u32 s68, s68, 0x20000\n"
        "s_xor_b32 s69, s60, 4\n"
        "s_mul_i32 s69, s69, 3072\n"
        "s_add_u32 s69, s69, 0x20000\n"
        "v_lshl_add_u32 v7, v1, 4, s68\n"
        "v_lshl_add_u32 v8, v1, 4, s69\n"
        "s_mov_b32 s74, s78\n"
        "s_add_u32 s75, s78, 0x1000\n"
        "s_waitcnt vmcnt(0)\n"
        "v_readfirstlane_b32 s68, v252\n"
        "v_readfirstlane_b32 s69, v253\n"
        "s_mul_i32 s68, s68, 0x9e3779b1\n"
        "s_add_u32 s64, s68, 0x7f4a7c15\n"
        "s_mul_i32 s69, s69, 0x85ebca6d\n"
        "s_add_u32 s65, s69, 0xc2b2ae3d\n"
        "s_barrier\n"
        "ds_read_b128 v[48:51], v236 offset:0\n"
        "ds_read_b128 v[52:55], v236 offset:2048\n"
        "ds_read_b64_tr_b16 v[76:77], v238 offset:8192\n"
        "ds_read_b64_tr_b16 v[78:79], v238 offset:8704\n"
        "ds_read_b64_tr_b16 v[80:81], v238 offset:16384\n"
        "ds_read_b64_tr_b16 v[82:83], v238 offset:16896\n"
        "ds_read_b64_tr_b16 v[72:73], v238 offset:0\n"
        "ds_read_b64_tr_b16 v[74:75], v238 offset:512\n"
        "ds_read_b128 v[56:59], v236 offset:4096\n"
        "ds_read_b128 v[60:63], v236 offset:6144\n"
        "s_waitcnt lgkmcnt(6)\n"
        "v_mfma_f32_16x16x32_bf16 v[16:19], v[76:79], v[48:51], 0\n"
        "v_mfma_f32_16x16x32_bf16 v[20:23], v[76:79], v[52:55], 0\n"
        "s_waitcnt lgkmcnt(4)\n"
        "v_mfma_f32_16x16x32_bf16 v[32:35], v[48:51], v[80:83], 0\n"
        "v_mfma_f32_16x16x32_bf16 v[36:39], v[52:55], v[80:83], 0\n"
        "s_waitcnt lgkmcnt(2)\n"
        "s_nop 0\n"
        "v_mfma_f32_16x16x32_bf16 v[96:99], v[72:75], v[48:51], 0\n"
        "v_mfma_f32_16x16x32_bf16 v[100:103], v[72:75], v[52:55], 0\n"
        "ds_read_b128 v[64:67], v237 offset:0\n"
        "ds_read_b128 v[68:71], v237 offset:2048\n"
        "ds_read_b64_tr_b16 v[88:89], v238 offset:12288\n"
        "ds_read_b64_tr_b16 v[90:91], v238 offset:12800\n"
        "ds_read_b64_tr_b16 v[92:93], v238 offset:20480\n"
        "ds_read_b64_tr_b16 v[94:95], v238 offset:20992\n"
        "ds_read_b64_tr_b16 v[84:85], v238 offset:4096\n"
        "ds_read_b64_tr_b16 v[86:87], v238 offset:4608\n"
        "s_waitcnt lgkmcnt(8)\n"
        "s_nop 0\n"
        "v_mfma_f32_16x16x32_bf16 v[24:27], v[76:79], v[56:59], 0\n"
        "v_mfma_f32_16x16x32_bf16 v[28:31], v[76:79], v[60:63], 0\n"
        "v_mfma_f32_16x16x32_bf16 v[40:43], v[56:59], v[80:83], 0\n"
        "v_mfma_f32_16x16x32_bf16 v[44:47], v[60:63], v[80:83], 0\n"
        "v_mfma_f32_16x16x32_bf16 v[104:107], v[72:75], v[56:59], 0\n"
        "v_mfma_f32_16x16x32_bf16 v[108:111], v[72:75], v[60:63], 0\n"
        "ds_read_b128 v[48:51], v237 offset:4096\n"
        "ds_read_b128 v[52:55], v237 offset:6144\n"
        "s_waitcnt lgkmcnt(6)\n"
        "v_mfma_f32_16x16x32_bf16 v[16:19], v[88:91], v[64:67], v[16:19]\n"
        "v_mfma_f32_16x16x32_bf16 v[20:23], v[88:91], v[68:71], v[20:23]\n"
        "s_waitcnt lgkmcnt(4)\n"
        "v_mfma_f32_16x16x32_bf16 v[32:35], v[64:67], v[92:95], v[32:35]\n"
        "v_mfma_f32_16x16x32_bf16 v[36:39], v[68:71], v[92:95], v[36:39]\n"
        "s_waitcnt lgkmcnt(2)\n"
        "v_mfma_f32_16x16x32_bf16 v[96:99], v[84:87], v[64:67], v[96:99]\n"
        "v_mfma_f32_16x16x32_bf16 v[100:103], v[84:87], v[68:71], v[100:103]\n"
        "s_barrier\n"
        "ds_read_b128 v[56:59], v236 offset:32768\n"
        "ds_read_b128 v[60:63], v236 offset:34816\n"
        "ds_read_b64_tr_b16 v[76:77], v238 offset:40960\n"
        "ds_read_b64_tr_b16 v[78:79], v238 offset:41472\n"
        "ds_read_b64_tr_b16 v[80:81], v238 offset:49152\n"
        "ds_read_b64_tr_b16 v[82:83], v238 offset:49664\n"
        "ds_read_b64_tr_b16 v[72:73], v238 offset:32768\n"
        "ds_read_b64_tr_b16 v[74:75], v238 offset:33280\n"
        "s_waitcnt lgkmcnt(8)\n"
        "s_nop 0\n"
        "v_mfma_f32_16x16x32_bf16 v[24:27], v[88:91], v[48:51], v[24:27]\n"
        "v_mfma_f32_16x16x32_bf16 v[28:31], v[88:91], v[52:55], v[28:31]\n"
        "v_mfma_f32_16x16x32_bf16 v[40:43], v[48:51], v[92:95], v[40:43]\n"
        "v_mfma_f32_16x16x32_bf16 v[44:47], v[52:55], v[92:95], v[44:47]\n"
        "v_mfma_f32_16x16x32_bf16 v[104:107], v[84:87], v[48:51], v[104:107]\n"
        "v_mfma_f32_16x16x32_bf16 v[108:111], v[84:87], v[52:55], v[108:111]\n"
        "ds_read_b128 v[64:67], v236 offset:36864\n"
        "ds_read_b128 v[68:71], v236 offset:38912\n"
        "s_waitcnt lgkmcnt(6)\n"
        "v_mfma_f32_16x16x32_bf16 v[16:19], v[76:79], v[56:59], v[16:19]\n"
        "v_mfma_f32_16x16x32_bf16 v[20:23], v[76:79], v[60:63], v[20:23]\n"
        "s_waitcnt lgkmcnt(4)\n"
        "v_mfma_f32_16x16x32_bf16 v[32:35], v[56:59], v[80:83], v[32:35]\n"
        "v_mfma_f32_16x16x32_bf16 v[36:39], v[60:63], v[80:83], v[36:39]\n"
        "s_waitcnt lgkmcnt(2)\n"
        "s_nop 0\n"
        "v_mfma_f32_16x16x32_bf16 v[96:99], v[72:75], v[56:59], v[96:99]\n"
        "v_mfma_f32_16x16x32_bf16 v[100:103], v[72:75], v[60:63], v[100:103]\n"
        "ds_read_b128 v[48:51], v237 offset:32768\n"
        "ds_read_b128 v[52:55], v237 offset:34816\n"
        "ds_read_b64_tr_b16 v[88:89], v238 offset:45056\n"
        "ds_read_b64_tr_b16 v[90:91], v238 offset:45568\n"
        "ds_read_b64_tr_b16 v[92:93], v238 offset:53248\n"
        "ds_read_b64_tr_b16 v[94:95], v238 offset:53760\n"
        "ds_read_b64_tr_b16 v[84:85], v238 offset:36864\n"
        "ds_read_b64_tr_b16 v[86:87], v238 offset:37376\n"
        "s_waitcnt lgkmcnt(8)\n"
        "s_nop 0\n"
        "v_mfma_f32_16x16x32_bf16 v[24:27], v[76:79], v[64:67], v[24:27]\n"
        "v_mfma_f32_16x16x32_bf16 v[28:31], v[76:79], v[68:71], v[28:31]\n"
        "v_mfma_f32_16x16x32_bf16 v[40:43], v[64:67], v[80:83], v[40:43]\n"
        "v_mfma_f32_16x16x32_bf16 v[44:47], v[68:71], v[80:83], v[44:47]\n"
        "v_mfma_f32_16x16x32_bf16 v[104:107], v[72:75], v[64:67], v[104:107]\n"
        "v_mfma_f32_16x16x32_bf16 v[108:111], v[72:75], v[68:71], v[108:111]\n"
        "ds_read_b128 v[56:59], v237 offset:36864\n"
        "ds_read_b128 v[60:63], v237 offset:38912\n"
        "s_waitcnt lgkmcnt(6)\n"
        "v_mfma_f32_16x16x32_bf16 v[16:19], v[88:91], v[48:51], v[16:19]\n"
        "v_mfma_f32_16x16x32_bf16 v[20:23], v[88:91], v[52:55], v[20:23]\n"
        "s_waitcnt lgkmcnt(4)\n"
        "v_mfma_f32_16x16x32_bf16 v[32:35], v[48:51], v[92:95], v[32:35]\n"
        "v_mfma_f32_16x16x32_bf16 v[36:39], v[52:55], v[92:95], v[36:39]\n"
        "s_waitcnt lgkmcnt(2)\n"
        "v_mfma_f32_16x16x32_bf16 v[96:99], v[84:87], v[48:51], v[96:99]\n"
        "v_mfma_f32_16x16x32_bf16 v[100:103], v[84:87], v[52:55], v[100:103]\n"
        "s_barrier\n"
        "v_xor_b32 v236, 0x10000, v236\n"
        "v_xor_b32 v237, 0x10000, v237\n"
        "v_xor_b32 v238, 0x10000, v238\n"
        "ds_read_b128 v[64:67], v236 offset:0\n"
        "ds_read_b128 v[68:71], v236 offset:2048\n"
        "ds_read_b64_tr_b16 v[76:77], v238 offset:8192\n"
        "ds_read_b64_tr_b16 v[78:79], v238 offset:8704\n"
        "ds_read_b64_tr_b16 v[80:81], v238 offset:16384\n"
        "ds_read_b64_tr_b16 v[82:83], v238 offset:16896\n"
        "ds_read_b64_tr_b16 v[72:73], v238 offset:0\n"
        "ds_read_b64_tr_b16 v[74:75], v238 offset:512\n"
        "s_waitcnt lgkmcnt(8)\n"
        "s_nop 0\n"
        "v_mfma_f32_16x16x32_bf16 v[24:27], v[88:91], v[56:59], v[24:27]\n"
        "v_mfma_f32_16x16x32_bf16 v[28:31], v[88:91], v[60:63], v[28:31]\n"
        "v_mfma_f32_16x16x32_bf16 v[40:43], v[56:59], v[92:95], v[40:43]\n"
        "v_mfma_f32_16x16x32_bf16 v[44:47], v[60:63], v[92:95], v[44:47]\n"
        "v_mfma_f32_16x16x32_bf16 v[104:107], v[84:87], v[56:59], v[104:107]\n"
        "v_mfma_f32_16x16x32_bf16 v[108:111], v[84:87], v[60:63], v[108:111]\n"
        "ds_read_b128 v[48:51], v236 offset:4096\n"
        "ds_read_b128 v[52:55], v236 offset:6144\n"
        "s_waitcnt lgkmcnt(6)\n"
        "v_mfma_f32_16x16x32_bf16 v[16:19], v[76:79], v[64:67], v[16:19]\n"
        "v_mfma_f32_16x16x32_bf16 v[20:23], v[76:79], v[68:71], v[20:23]\n"
        "s_waitcnt lgkmcnt(4)\n"
        "v_mfma_f32_16x16x32_bf16 v[32:35], v[64:67], v[80:83], v[32:35]\n"
        "v_mfma_f32_16x16x32_bf16 v[36:39], v[68:71], v[80:83], v[36:39]\n"
        "s_waitcnt lgkmcnt(2)\n"
        "s_nop 0\n"
        "v_mfma_f32_16x16x32_bf16 v[96:99], v[72:75], v[64:67], v[96:99]\n"
        "v_mfma_f32_16x16x32_bf16 v[100:103], v[72:75], v[68:71], v[100:103]\n"
        "ds_read_b128 v[56:59], v237 offset:0\n"
        "ds_read_b128 v[60:63], v237 offset:2048\n"
        "ds_read_b64_tr_b16 v[88:89], v238 offset:12288\n"
        "ds_read_b64_tr_b16 v[90:91], v238 offset:12800\n"
        "ds_read_b64_tr_b16 v[92:93], v238 offset:20480\n"
        "ds_read_b64_tr_b16 v[94:95], v238 offset:20992\n"
        "ds_read_b64_tr_b16 v[84:85], v238 offset:4096\n"
        "ds_read_b64_tr_b16 v[86:87], v238 offset:4608\n"
        "s_waitcnt lgkmcnt(8)\n"
        "s_nop 0\n"
        "v_mfma_f32_16x16x32_bf16 v[24:27], v[76:79], v[48:51], v[24:27]\n"
        "v_mfma_f32_16x16x32_bf16 v[28:31], v[76:79], v[52:55], v[28:31]\n"
        "v_mfma_f32_16x16x32_bf16 v[40:43], v[48:51], v[80:83], v[40:43]\n"
        "v_mfma_f32_16x16x32_bf16 v[44:47], v[52:55], v[80:83], v[44:47]\n"
        "v_mfma_f32_16x16x32_bf16 v[104:107], v[72:75], v[48:51], v[104:107]\n"
        "v_mfma_f32_16x16x32_bf16 v[108:111], v[72:75], v[52:55], v[108:111]\n"
        "ds_read_b128 v[64:67], v237 offset:4096\n"
        "ds_read_b128 v[68:71], v237 offset:6144\n"
        "s_waitcnt lgkmcnt(6)\n"
        "v_mfma_f32_16x16x32_bf16 v[16:19], v[88:91], v[56:59], v[16:19]\n"
        "v_mfma_f32_16x16x32_bf16 v[20:23], v[88:91], v[60:63], v[20:23]\n"
        "s_waitcnt lgkmcnt(4)\n"
        "v_mfma_f32_16x16x32_bf16 v[32:35], v[56:59], v[92:95], v[32:35]\n"
        "v_mfma_f32_16x16x32_bf16 v[36:39], v[60:63], v[92:95], v[36:39]\n"
        "s_waitcnt lgkmcnt(2)\n"
        "v_mfma_f32_16x16x32_bf16 v[96:99], v[84:87], v[56:59], v[96:99]\n"
        "v_mfma_f32_16x16x32_bf16 v[100:103], v[84:87], v[60:63], v[100:103]\n"
        "s_barrier\n"
        "ds_read_b128 v[48:51], v236 offset:32768\n"
        "ds_read_b128 v[52:55], v236 offset:34816\n"
        "ds_read_b64_tr_b16 v[76:77], v238 offset:40960\n"
        "ds_read_b64_tr_b16 v[78:79], v238 offset:41472\n"
        "ds_read_b64_tr_b16 v[80:81], v238 offset:49152\n"
        "ds_read_b64_tr_b16 v[82:83], v238 offset:49664\n"
        "ds_read_b64_tr_b16 v[72:73], v238 offset:32768\n"
        "ds_read_b64_tr_b16 v[74:75], v238 offset:33280\n"
        "s_waitcnt lgkmcnt(8)\n"
        "s_nop 0\n"
        "v_mfma_f32_16x16x32_bf16 v[24:27], v[88:91], v[64:67], v[24:27]\n"
        "v_mfma_f32_16x16x32_bf16 v[28:31], v[88:91], v[68:71], v[28:31]\n"
        "v_mfma_f32_16x16x32_bf16 v[40:43], v[64:67], v[92:95], v[40:43]\n"
        "v_mfma_f32_16x16x32_bf16 v[44:47], v[68:71], v[92:95], v[44:47]\n"
        "v_mfma_f32_16x16x32_bf16 v[104:107], v[84:87], v[64:67], v[104:107]\n"
        "v_mfma_f32_16x16x32_bf16 v[108:111], v[84:87], v[68:71], v[108:111]\n"
        "ds_read_b128 v[56:59], v236 offset:36864\n"
        "ds_read_b128 v[60:63], v236 offset:38912\n"
        "s_waitcnt lgkmcnt(6)\n"
        "v_mfma_f32_16x16x32_bf16 v[16:19], v[76:79], v[48:51], v[16:19]\n"
        "v_mfma_f32_16x16x32_bf16 v[20:23], v[76:79], v[52:55], v[20:23]\n"
        "s_waitcnt lgkmcnt(4)\n"
        "v_mfma_f32_16x16x32_bf16 v[32:35], v[48:51], v[80:83], v[32:35]\n"
        "v_mfma_f32_16x16x32_bf16 v[36:39], v[52:55], v[80:83], v[36:39]\n"
        "s_waitcnt lgkmcnt(2)\n"
        "s_nop 0\n"
        "v_mfma_f32_16x16x32_bf16 v[96:99], v[72:75], v[48:51], v[96:99]\n"
        "v_mfma_f32_16x16x32_bf16 v[100:103], v[72:75], v[52:55], v[100:103]\n"
        "ds_read_b128 v[64:67], v237 offset:32768\n"
        "ds_read_b128 v[68:71], v237 offset:34816\n"
        "ds_read_b64_tr_b16 v[88:89], v238 offset:45056\n"
        "ds_read_b64_tr_b16 v[90:91], v238 offset:45568\n"
        "ds_read_b64_tr_b16 v[92:93], v238 offset:53248\n"
        "ds_read_b64_tr_b16 v[94:95], v238 offset:53760\n"
        "ds_read_b64_tr_b16 v[84:85], v238 offset:36864\n"
        "ds_read_b64_tr_b16 v[86:87], v238 offset:37376\n"
        "s_waitcnt lgkmcnt(8)\n"
        "s_nop 0\n"
        "v_mfma_f32_16x16x32_bf16 v[24:27], v[76:79], v[56:59], v[24:27]\n"
        "v_mfma_f32_16x16x32_bf16 v[28:31], v[76:79], v[60:63], v[28:31]\n"
        "v_mfma_f32_16x16x32_bf16 v[40:43], v[56:59], v[80:83], v[40:43]\n"
        "v_mfma_f32_16x16x32_bf16 v[44:47], v[60:63], v[80:83], v[44:47]\n"
        "v_mfma_f32_16x16x32_bf16 v[104:107], v[72:75], v[56:59], v[104:107]\n"
        "v_mfma_f32_16x16x32_bf16 v[108:111], v[72:75], v[60:63], v[108:111]\n"
        "ds_read_b128 v[48:51], v237 offset:36864\n"
        "ds_read_b128 v[52:55], v237 offset:38912\n"
        "s_waitcnt lgkmcnt(6)\n"
        "v_mfma_f32_16x16x32_bf16 v[16:19], v[88:91], v[64:67], v[16:19]\n"
        "v_mfma_f32_16x16x32_bf16 v[20:23], v[88:91], v[68:71], v[20:23]\n"
        "s_waitcnt lgkmcnt(4)\n"
        "v_mfma_f32_16x16x32_bf16 v[32:35], v[64:67], v[92:95], v[32:35]\n"
        "v_mfma_f32_16x16x32_bf16 v[36:39], v[68:71], v[92:95], v[36:39]\n"
        "s_waitcnt lgkmcnt(2)\n"
        "v_mfma_f32_16x16x32_bf16 v[96:99], v[84:87], v[64:67], v[96:99]\n"
        "v_mfma_f32_16x16x32_bf16 v[100:103], v[84:87], v[68:71], v[100:103]\n"
        "s_barrier\n"
        "v_xor_b32 v236, 0x10000, v236\n"
        "v_xor_b32 v237, 0x10000, v237\n"
        "v_xor_b32 v238, 0x10000, v238\n"
        "ds_read_b128 v[56:59], v236 offset:0\n"
        "ds_read_b128 v[60:63], v236 offset:2048\n"
        "ds_read_b64_tr_b16 v[76:77], v238 offset:8192\n"
        "ds_read_b64_tr_b16 v[78:79], v238 offset:8704\n"
        "ds_read_b64_tr_b16 v[80:81], v238 offset:16384\n"
        "ds_read_b64_tr_b16 v[82:83], v238 offset:16896\n"
        "ds_read_b64_tr_b16 v[72:73], v238 offset:0\n"
        "ds_read_b64_tr_b16 v[74:75], v238 offset:512\n"
        "s_waitcnt lgkmcnt(8)\n"
        "s_nop 0\n"
        "v_mfma_f32_16x16x32_bf16 v[24:27], v[88:91], v[48:51], v[24:27]\n"
        "v_mfma_f32_16x16x32_bf16 v[28:31], v[88:91], v[52:55], v[28:31]\n"
        "v_mfma_f32_16x16x32_bf16 v[40:43], v[48:51], v[92:95], v[40:43]\n"
        "v_mfma_f32_16x16x32_bf16 v[44:47], v[52:55], v[92:95], v[44:47]\n"
        "v_mfma_f32_16x16x32_bf16 v[104:107], v[84:87], v[48:51], v[104:107]\n"
        "v_mfma_f32_16x16x32_bf16 v[108:111], v[84:87], v[52:55], v[108:111]\n"
        "ds_read_b128 v[64:67], v236 offset:4096\n"
        "ds_read_b128 v[68:71], v236 offset:6144\n"
        "s_waitcnt lgkmcnt(6)\n"
        "v_mfma_f32_16x16x32_bf16 v[16:19], v[76:79], v[56:59], v[16:19]\n"
        "v_mfma_f32_16x16x32_bf16 v[20:23], v[76:79], v[60:63], v[20:23]\n"
        "s_waitcnt lgkmcnt(4)\n"
        "v_mfma_f32_16x16x32_bf16 v[32:35], v[56:59], v[80:83], v[32:35]\n"
        "v_mfma_f32_16x16x32_bf16 v[36:39], v[60:63], v[80:83], v[36:39]\n"
        "s_waitcnt lgkmcnt(2)\n"
        "s_nop 0\n"
        "v_mfma_f32_16x16x32_bf16 v[96:99], v[72:75], v[56:59], v[96:99]\n"
        "v_mfma_f32_16x16x32_bf16 v[100:103], v[72:75], v[60:63], v[100:103]\n"
        "ds_read_b128 v[48:51], v237 offset:0\n"
        "ds_read_b128 v[52:55], v237 offset:2048\n"
        "ds_read_b64_tr_b16 v[88:89], v238 offset:12288\n"
        "ds_read_b64_tr_b16 v[90:91], v238 offset:12800\n"
        "ds_read_b64_tr_b16 v[92:93], v238 offset:20480\n"
        "ds_read_b64_tr_b16 v[94:95], v238 offset:20992\n"
        "ds_read_b64_tr_b16 v[84:85], v238 offset:4096\n"
        "ds_read_b64_tr_b16 v[86:87], v238 offset:4608\n"
        "s_waitcnt lgkmcnt(8)\n"
        "s_nop 0\n"
        "v_mfma_f32_16x16x32_bf16 v[24:27], v[76:79], v[64:67], v[24:27]\n"
        "v_mfma_f32_16x16x32_bf16 v[28:31], v[76:79], v[68:71], v[28:31]\n"
        "v_mfma_f32_16x16x32_bf16 v[40:43], v[64:67], v[80:83], v[40:43]\n"
        "v_mfma_f32_16x16x32_bf16 v[44:47], v[68:71], v[80:83], v[44:47]\n"
        "v_mfma_f32_16x16x32_bf16 v[104:107], v[72:75], v[64:67], v[104:107]\n"
        "v_mfma_f32_16x16x32_bf16 v[108:111], v[72:75], v[68:71], v[108:111]\n"
        "ds_read_b128 v[56:59], v237 offset:4096\n"
        "ds_read_b128 v[60:63], v237 offset:6144\n"
        "s_waitcnt lgkmcnt(6)\n"
        "v_mfma_f32_16x16x32_bf16 v[16:19], v[88:91], v[48:51], v[16:19]\n"
        "v_mfma_f32_16x16x32_bf16 v[20:23], v[88:91], v[52:55], v[20:23]\n"
        "s_waitcnt lgkmcnt(4)\n"
        "v_mfma_f32_16x16x32_bf16 v[32:35], v[48:51], v[92:95], v[32:35]\n"
        "v_mfma_f32_16x16x32_bf16 v[36:39], v[52:55], v[92:95], v[36:39]\n"
        "s_waitcnt lgkmcnt(2)\n"
        "v_mfma_f32_16x16x32_bf16 v[96:99], v[84:87], v[48:51], v[96:99]\n"
        "v_mfma_f32_16x16x32_bf16 v[100:103], v[84:87], v[52:55], v[100:103]\n"
        "s_barrier\n"
        "ds_read_b128 v[64:67], v236 offset:32768\n"
        "ds_read_b128 v[68:71], v236 offset:34816\n"
        "ds_read_b64_tr_b16 v[76:77], v238 offset:40960\n"
        "ds_read_b64_tr_b16 v[78:79], v238 offset:41472\n"
        "ds_read_b64_tr_b16 v[80:81], v238 offset:49152\n"
        "ds_read_b64_tr_b16 v[82:83], v238 offset:49664\n"
        "ds_read_b64_tr_b16 v[72:73], v238 offset:32768\n"
        "ds_read_b64_tr_b16 v[74:75], v238 offset:33280\n"
        "s_waitcnt lgkmcnt(8)\n"
        "s_nop 0\n"
        "v_mfma_f32_16x16x32_bf16 v[24:27], v[88:91], v[56:59], v[24:27]\n"
        "v_mfma_f32_16x16x32_bf16 v[28:31], v[88:91], v[60:63], v[28:31]\n"
        "v_mfma_f32_16x16x32_bf16 v[40:43], v[56:59], v[92:95], v[40:43]\n"
        "v_mfma_f32_16x16x32_bf16 v[44:47], v[60:63], v[92:95], v[44:47]\n"
        "v_mfma_f32_16x16x32_bf16 v[104:107], v[84:87], v[56:59], v[104:107]\n"
        "v_mfma_f32_16x16x32_bf16 v[108:111], v[84:87], v[60:63], v[108:111]\n"
        "ds_read_b128 v[48:51], v236 offset:36864\n"
        "ds_read_b128 v[52:55], v236 offset:38912\n"
        "s_waitcnt lgkmcnt(6)\n"
        "v_mfma_f32_16x16x32_bf16 v[16:19], v[76:79], v[64:67], v[16:19]\n"
        "v_mfma_f32_16x16x32_bf16 v[20:23], v[76:79], v[68:71], v[20:23]\n"
        "s_waitcnt lgkmcnt(4)\n"
        "v_mfma_f32_16x16x32_bf16 v[32:35], v[64:67], v[80:83], v[32:35]\n"
        "v_mfma_f32_16x16x32_bf16 v[36:39], v[68:71], v[80:83], v[36:39]\n"
        "s_waitcnt lgkmcnt(2)\n"
        "s_nop 0\n"
        "v_mfma_f32_16x16x32_bf16 v[96:99], v[72:75], v[64:67], v[96:99]\n"
        "v_mfma_f32_16x16x32_bf16 v[100:103], v[72:75], v[68:71], v[100:103]\n"
        "ds_read_b128 v[56:59], v237 offset:32768\n"
        "ds_read_b128 v[60:63], v237 offset:34816\n"
        "ds_read_b64_tr_b16 v[88:89], v238 offset:45056\n"
        "ds_read_b64_tr_b16 v[90:91], v238 offset:45568\n"
        "ds_read_b64_tr_b16 v[92:93], v238 offset:53248\n"
        "ds_read_b64_tr_b16 v[94:95], v238 offset:53760\n"
        "ds_read_b64_tr_b16 v[84:85], v238 offset:36864\n"
        "ds_read_b64_tr_b16 v[86:87], v238 offset:37376\n"
        "s_waitcnt lgkmcnt(8)\n"
        "s_nop 0\n"
        "v_mfma_f32_16x16x32_bf16 v[24:27], v[76:79], v[48:51], v[24:27]\n"
        "v_mfma_f32_16x16x32_bf16 v[28:31], v[76:79], v[52:55], v[28:31]\n"
        "v_mfma_f32_16x16x32_bf16 v[40:43], v[48:51], v[80:83], v[40:43]\n"
        "v_mfma_f32_16x16x32_bf16 v[44:47], v[52:55], v[80:83], v[44:47]\n"
        "v_mfma_f32_16x16x32_bf16 v[104:107], v[72:75], v[48:51], v[104:107]\n"
        "v_mfma_f32_16x16x32_bf16 v[108:111], v[72:75], v[52:55], v[108:111]\n"
        "ds_read_b128 v[64:67], v237 offset:36864\n"
        "ds_read_b128 v[68:71], v237 offset:38912\n"
        "s_waitcnt lgkmcnt(6)\n"
        "v_mfma_f32_16x16x32_bf16 v[16:19], v[88:91], v[56:59], v[16:19]\n"
        "v_mfma_f32_16x16x32_bf16 v[20:23], v[88:91], v[60:63], v[20:23]\n"
        "s_waitcnt lgkmcnt(4)\n"
        "v_mfma_f32_16x16x32_bf16 v[32:35], v[56:59], v[92:95], v[32:35]\n"
        "v_mfma_f32_16x16x32_bf16 v[36:39], v[60:63], v[92:95], v[36:39]\n"
        "s_waitcnt lgkmcnt(2)\n"
        "v_mfma_f32_16x16x32_bf16 v[96:99], v[84:87], v[56:59], v[96:99]\n"
        "v_mfma_f32_16x16x32_bf16 v[100:103], v[84:87], v[60:63], v[100:103]\n"
        "s_barrier\n"
        "v_xor_b32 v236, 0x10000, v236\n"
        "v_xor_b32 v237, 0x10000, v237\n"
        "v_xor_b32 v238, 0x10000, v238\n"
        "ds_read_b128 v[48:51], v236 offset:0\n"
        "ds_read_b128 v[52:55], v236 offset:2048\n"
        "ds_read_b64_tr_b16 v[76:77], v238 offset:8192\n"
        "ds_read_b64_tr_b16 v[78:79], v238 offset:8704\n"
        "ds_read_b64_tr_b16 v[80:81], v238 offset:16384\n"
        "ds_read_b64_tr_b16 v[82:83], v238 offset:16896\n"
        "ds_read_b64_tr_b16 v[72:73], v238 offset:0\n"
        "ds_read_b64_tr_b16 v[74:75], v238 offset:512\n"
        "s_waitcnt lgkmcnt(8)\n"
        "s_nop 0\n"
        "v_mfma_f32_16x16x32_bf16 v[24:27], v[88:91], v[64:67], v[24:27]\n"
        "v_mfma_f32_16x16x32_bf16 v[28:31], v[88:91], v[68:71], v[28:31]\n"
        "v_mfma_f32_16x16x32_bf16 v[40:43], v[64:67], v[92:95], v[40:43]\n"
        "v_mfma_f32_16x16x32_bf16 v[44:47], v[68:71], v[92:95], v[44:47]\n"
        "v_mfma_f32_16x16x32_bf16 v[104:107], v[84:87], v[64:67], v[104:107]\n"
        "v_mfma_f32_16x16x32_bf16 v[108:111], v[84:87], v[68:71], v[108:111]\n"
        "ds_read_b128 v[56:59], v236 offset:4096\n"
        "ds_read_b128 v[60:63], v236 offset:6144\n"
        "s_waitcnt lgkmcnt(6)\n"
        "v_mfma_f32_16x16x32_bf16 v[16:19], v[76:79], v[48:51], v[16:19]\n"
        "v_mfma_f32_16x16x32_bf16 v[20:23], v[76:79], v[52:55], v[20:23]\n"
        "s_waitcnt lgkmcnt(4)\n"
        "v_mfma_f32_16x16x32_bf16 v[32:35], v[48:51], v[80:83], v[32:35]\n"
        "v_mfma_f32_16x16x32_bf16 v[36:39], v[52:55], v[80:83], v[36:39]\n"
        "s_waitcnt lgkmcnt(2)\n"
        "s_nop 0\n"
        "v_mfma_f32_16x16x32_bf16 v[96:99], v[72:75], v[48:51], v[96:99]\n"
        "v_mfma_f32_16x16x32_bf16 v[100:103], v[72:75], v[52:55], v[100:103]\n"
        "ds_read_b128 v[64:67], v237 offset:0\n"
        "ds_read_b128 v[68:71], v237 offset:2048\n"
        "ds_read_b64_tr_b16 v[88:89], v238 offset:12288\n"
        "ds_read_b64_tr_b16 v[90:91], v238 offset:12800\n"
        "ds_read_b64_tr_b16 v[92:93], v238 offset:20480\n"
        "ds_read_b64_tr_b16 v[94:95], v238 offset:20992\n"
        "ds_read_b64_tr_b16 v[84:85], v238 offset:4096\n"
        "ds_read_b64_tr_b16 v[86:87], v238 offset:4608\n"
        "s_waitcnt lgkmcnt(8)\n"
        "s_nop 0\n"
        "v_mfma_f32_16x16x32_bf16 v[24:27], v[76:79], v[56:59], v[24:27]\n"
        "v_mfma_f32_16x16x32_bf16 v[28:31], v[76:79], v[60:63], v[28:31]\n"
        "v_mfma_f32_16x16x32_bf16 v[40:43], v[56:59], v[80:83], v[40:43]\n"
        "v_mfma_f32_16x16x32_bf16 v[44:47], v[60:63], v[80:83], v[44:47]\n"
        "v_mfma_f32_16x16x32_bf16 v[104:107], v[72:75], v[56:59], v[104:107]\n"
        "v_mfma_f32_16x16x32_bf16 v[108:111], v[72:75], v[60:63], v[108:111]\n"
        "ds_read_b128 v[48:51], v237 offset:4096\n"
        "ds_read_b128 v[52:55], v237 offset:6144\n"
        "s_waitcnt lgkmcnt(6)\n"
        "v_mfma_f32_16x16x32_bf16 v[16:19], v[88:91], v[64:67], v[16:19]\n"
        "v_mfma_f32_16x16x32_bf16 v[20:23], v[88:91], v[68:71], v[20:23]\n"
        "s_waitcnt lgkmcnt(4)\n"
        "v_mfma_f32_16x16x32_bf16 v[32:35], v[64:67], v[92:95], v[32:35]\n"
        "v_mfma_f32_16x16x32_bf16 v[36:39], v[68:71], v[92:95], v[36:39]\n"
        "s_waitcnt lgkmcnt(2)\n"
        "v_mfma_f32_16x16x32_bf16 v[96:99], v[84:87], v[64:67], v[96:99]\n"
        "v_mfma_f32_16x16x32_bf16 v[100:103], v[84:87], v[68:71], v[100:103]\n"
        "s_barrier\n"
        "ds_read_b128 v[56:59], v236 offset:32768\n"
        "ds_read_b128 v[60:63], v236 offset:34816\n"
        "ds_read_b64_tr_b16 v[76:77], v238 offset:40960\n"
        "ds_read_b64_tr_b16 v[78:79], v238 offset:41472\n"
        "ds_read_b64_tr_b16 v[80:81], v238 offset:49152\n"
        "ds_read_b64_tr_b16 v[82:83], v238 offset:49664\n"
        "ds_read_b64_tr_b16 v[72:73], v238 offset:32768\n"
        "ds_read_b64_tr_b16 v[74:75], v238 offset:33280\n"
        "s_waitcnt lgkmcnt(8)\n"
        "s_nop 0\n"
        "v_mfma_f32_16x16x32_bf16 v[24:27], v[88:91], v[48:51], v[24:27]\n"
        "v_mfma_f32_16x16x32_bf16 v[28:31], v[88:91], v[52:55], v[28:31]\n"
        "v_mfma_f32_16x16x32_bf16 v[40:43], v[48:51], v[92:95], v[40:43]\n"
        "v_mfma_f32_16x16x32_bf16 v[44:47], v[52:55], v[92:95], v[44:47]\n"
        "v_mfma_f32_16x16x32_bf16 v[104:107], v[84:87], v[48:51], v[104:107]\n"
        "v_mfma_f32_16x16x32_bf16 v[108:111], v[84:87], v[52:55], v[108:111]\n"
        "ds_read_b128 v[64:67], v236 offset:36864\n"
        "ds_read_b128 v[68:71], v236 offset:38912\n"
        "s_waitcnt lgkmcnt(6)\n"
        "v_mfma_f32_16x16x32_bf16 v[16:19], v[76:79], v[56:59], v[16:19]\n"
        "v_mfma_f32_16x16x32_bf16 v[20:23], v[76:79], v[60:63], v[20:23]\n"
        "s_waitcnt lgkmcnt(4)\n"
        "v_mfma_f32_16x16x32_bf16 v[32:35], v[56:59], v[80:83], v[32:35]\n"
        "v_mfma_f32_16x16x32_bf16 v[36:39], v[60:63], v[80:83], v[36:39]\n"
        "s_waitcnt lgkmcnt(2)\n"
        "s_nop 0\n"
        "v_mfma_f32_16x16x32_bf16 v[96:99], v[72:75], v[56:59], v[96:99]\n"
        "v_mfma_f32_16x16x32_bf16 v[100:103], v[72:75], v[60:63], v[100:103]\n"
        "ds_read_b128 v[48:51], v237 offset:32768\n"
        "ds_read_b128 v[52:55], v237 offset:34816\n"
        "ds_read_b64_tr_b16 v[88:89], v238 offset:45056\n"
        "ds_read_b64_tr_b16 v[90:91], v238 offset:45568\n"
        "ds_read_b64_tr_b16 v[92:93], v238 offset:53248\n"
        "ds_read_b64_tr_b16 v[94:95], v238 offset:53760\n"
        "ds_read_b64_tr_b16 v[84:85], v238 offset:36864\n"
        "ds_read_b64_tr_b16 v[86:87], v238 offset:37376\n"
        "s_waitcnt lgkmcnt(8)\n"
        "s_nop 0\n"
        "v_mfma_f32_16x16x32_bf16 v[24:27], v[76:79], v[64:67], v[24:27]\n"
        "v_mfma_f32_16x16x32_bf16 v[28:31], v[76:79], v[68:71], v[28:31]\n"
        "v_mfma_f32_16x16x32_bf16 v[40:43], v[64:67], v[80:83], v[40:43]\n"
        "v_mfma_f32_16x16x32_bf16 v[44:47], v[68:71], v[80:83], v[44:47]\n"
        "v_mfma_f32_16x16x32_bf16 v[104:107], v[72:75], v[64:67], v[104:107]\n"
        "v_mfma_f32_16x16x32_bf16 v[108:111], v[72:75], v[68:71], v[108:111]\n"
        "ds_read_b128 v[56:59], v237 offset:36864\n"
        "ds_read_b128 v[60:63], v237 offset:38912\n"
        "s_waitcnt lgkmcnt(6)\n"
        "v_mfma_f32_16x16x32_bf16 v[16:19], v[88:91], v[48:51], v[16:19]\n"
        "v_mfma_f32_16x16x32_bf16 v[20:23], v[88:91], v[52:55], v[20:23]\n"
        "s_waitcnt lgkmcnt(4)\n"
        "v_mfma_f32_16x16x32_bf16 v[32:35], v[48:51], v[92:95], v[32:35]\n"
        "v_mfma_f32_16x16x32_bf16 v[36:39], v[52:55], v[92:95], v[36:39]\n"
        "s_waitcnt lgkmcnt(2)\n"
        "v_mfma_f32_16x16x32_bf16 v[96:99], v[84:87], v[48:51], v[96:99]\n"
        "v_mfma_f32_16x16x32_bf16 v[100:103], v[84:87], v[52:55], v[100:103]\n"
        "s_waitcnt lgkmcnt(0)\n"
        "v_mfma_f32_16x16x32_bf16 v[24:27], v[88:91], v[56:59], v[24:27]\n"
        "v_mfma_f32_16x16x32_bf16 v[28:31], v[88:91], v[60:63], v[28:31]\n"
        "v_mfma_f32_16x16x32_bf16 v[40:43], v[56:59], v[92:95], v[40:43]\n"
        "v_mfma_f32_16x16x32_bf16 v[44:47], v[60:63], v[92:95], v[44:47]\n"
        "v_mfma_f32_16x16x32_bf16 v[104:107], v[84:87], v[56:59], v[104:107]\n"
        "v_mfma_f32_16x16x32_bf16 v[108:111], v[84:87], v[60:63], v[108:111]\n"
        "s_mov_b32 s68, 0x3eb8aa3b\n"
        "s_mov_b32 s69, 0x3eb8aa3b\n"
        "v_cvt_pk_bf16_f32 v32, v32, v33\n"
        "v_cvt_pk_bf16_f32 v33, v34, v35\n"
        "v_cvt_pk_bf16_f32 v34, v36, v37\n"
        "v_cvt_pk_bf16_f32 v35, v38, v39\n"
        "v_cvt_pk_bf16_f32 v36, v40, v41\n"
        "v_cvt_pk_bf16_f32 v37, v42, v43\n"
        "v_cvt_pk_bf16_f32 v38, v44, v45\n"
        "v_cvt_pk_bf16_f32 v39, v46, v47\n"
        "v_cvt_pk_bf16_f32 v40, v16, v17\n"
        "v_cvt_pk_bf16_f32 v41, v18, v19\n"
        "v_cvt_pk_bf16_f32 v42, v20, v21\n"
        "v_cvt_pk_bf16_f32 v43, v22, v23\n"
        "v_cvt_pk_bf16_f32 v44, v24, v25\n"
        "v_cvt_pk_bf16_f32 v45, v26, v27\n"
        "v_cvt_pk_bf16_f32 v46, v28, v29\n"
        "v_cvt_pk_bf16_f32 v47, v30, v31\n"
        "v_permlane16_swap_b32 v32, v34\n"
        "v_permlane16_swap_b32 v33, v35\n"
        "v_permlane16_swap_b32 v36, v38\n"
        "v_permlane16_swap_b32 v37, v39\n"
        "v_permlane16_swap_b32 v40, v42\n"
        "v_permlane16_swap_b32 v41, v43\n"
        "v_permlane16_swap_b32 v44, v46\n"
        "v_permlane16_swap_b32 v45, v47\n"
        "v_pk_mul_f32 v[96:97], v[96:97], s[68:69]\n"
        "v_pk_mul_f32 v[98:99], v[98:99], s[68:69]\n"
        "v_pk_mul_f32 v[100:101], v[100:101], s[68:69]\n"
        "v_pk_mul_f32 v[102:103], v[102:103], s[68:69]\n"
        "v_pk_mul_f32 v[104:105], v[104:105], s[68:69]\n"
        "v_pk_mul_f32 v[106:107], v[106:107], s[68:69]\n"
        "v_pk_mul_f32 v[108:109], v[108:109], s[68:69]\n"
        "v_pk_mul_f32 v[110:111], v[110:111], s[68:69]\n"
        "v_cvt_pk_bf16_f32 v180, v96, v97\n"
        "v_cvt_pk_bf16_f32 v181, v98, v99\n"
        "v_cvt_pk_bf16_f32 v182, v100, v101\n"
        "v_cvt_pk_bf16_f32 v183, v102, v103\n"
        "v_cvt_pk_bf16_f32 v176, v104, v105\n"
        "v_cvt_pk_bf16_f32 v177, v106, v107\n"
        "v_cvt_pk_bf16_f32 v178, v108, v109\n"
        "v_cvt_pk_bf16_f32 v179, v110, v111\n"
        "v_permlane16_swap_b32 v180, v182\n"
        "v_permlane16_swap_b32 v181, v183\n"
        "v_permlane16_swap_b32 v176, v178\n"
        "v_permlane16_swap_b32 v177, v179\n"
        "ds_write_b128 v226, v[180:183]\n"
        "ds_write_b128 v227, v[176:179]\n"
        "s_waitcnt lgkmcnt(0)\n"
        "s_barrier\n"
        "v_xor_b32 v236, 0x10000, v236\n"
        "v_xor_b32 v237, 0x10000, v237\n"
        "v_xor_b32 v238, 0x10000, v238\n"
        "ds_read_b128 v[64:67], v236 offset:0\n"
        "ds_read_b128 v[68:71], v236 offset:2048\n"
        "ds_read_b64_tr_b16 v[76:77], v238 offset:8192\n"
        "ds_read_b64_tr_b16 v[78:79], v238 offset:8704\n"
        "ds_read_b64_tr_b16 v[80:81], v238 offset:16384\n"
        "ds_read_b64_tr_b16 v[82:83], v238 offset:16896\n"
        "ds_read_b64_tr_b16 v[72:73], v238 offset:0\n"
        "ds_read_b64_tr_b16 v[74:75], v238 offset:512\n"
        "ds_read_b128 v[48:51], v236 offset:4096\n"
        "ds_read_b128 v[52:55], v236 offset:6144\n"
        "s_waitcnt lgkmcnt(6)\n"
        "v_mfma_f32_16x16x32_bf16 v[96:99], v[64:67], v[76:79], 0\n"
        "v_mfma_f32_16x16x32_bf16 v[100:103], v[68:71], v[76:79], 0\n"
        "s_waitcnt lgkmcnt(4)\n"
        "v_mfma_f32_16x16x32_bf16 v[112:115], v[64:67], v[80:83], 0\n"
        "v_mfma_f32_16x16x32_bf16 v[116:119], v[68:71], v[80:83], 0\n"
        "s_waitcnt lgkmcnt(2)\n"
        "s_nop 0\n"
        "v_mfma_f32_16x16x32_bf16 v[16:19], v[72:75], v[64:67], 0\n"
        "v_mfma_f32_16x16x32_bf16 v[20:23], v[72:75], v[68:71], 0\n"
        "ds_read_b128 v[56:59], v237 offset:0\n"
        "ds_read_b128 v[60:63], v237 offset:2048\n"
        "ds_read_b64_tr_b16 v[88:89], v238 offset:12288\n"
        "ds_read_b64_tr_b16 v[90:91], v238 offset:12800\n"
        "ds_read_b64_tr_b16 v[92:93], v238 offset:20480\n"
        "ds_read_b64_tr_b16 v[94:95], v238 offset:20992\n"
        "ds_read_b64_tr_b16 v[84:85], v238 offset:4096\n"
        "ds_read_b64_tr_b16 v[86:87], v238 offset:4608\n"
        "buffer_store_dwordx4 v[40:43], v240, s[52:55], s4 offen offset:0 sc1\n"
        "buffer_store_dwordx4 v[44:47], v240, s[52:55], s4 offen offset:2048 sc1\n"
        "s_waitcnt lgkmcnt(8)\n"
        "s_nop 0\n"
        "v_mfma_f32_16x16x32_bf16 v[104:107], v[48:51], v[76:79], 0\n"
        "v_mfma_f32_16x16x32_bf16 v[108:111], v[52:55], v[76:79], 0\n"
        "v_mfma_f32_16x16x32_bf16 v[120:123], v[48:51], v[80:83], 0\n"
        "v_mfma_f32_16x16x32_bf16 v[124:127], v[52:55], v[80:83], 0\n"
        "v_mfma_f32_16x16x32_bf16 v[24:27], v[72:75], v[48:51], 0\n"
        "v_mfma_f32_16x16x32_bf16 v[28:31], v[72:75], v[52:55], 0\n"
        "ds_read_b128 v[64:67], v237 offset:4096\n"
        "ds_read_b128 v[68:71], v237 offset:6144\n"
        "buffer_store_dwordx4 v[32:35], v241, s[52:55], s4 offen offset:1024 sc1\n"
        "buffer_store_dwordx4 v[36:39], v241, s[52:55], s4 offen offset:3072 sc1\n"
        "s_waitcnt lgkmcnt(6)\n"
        "v_mfma_f32_16x16x32_bf16 v[96:99], v[56:59], v[88:91], v[96:99]\n"
        "v_mfma_f32_16x16x32_bf16 v[100:103], v[60:63], v[88:91], v[100:103]\n"
        "s_waitcnt lgkmcnt(4)\n"
        "v_mfma_f32_16x16x32_bf16 v[112:115], v[56:59], v[92:95], v[112:115]\n"
        "v_mfma_f32_16x16x32_bf16 v[116:119], v[60:63], v[92:95], v[116:119]\n"
        "s_waitcnt lgkmcnt(2)\n"
        "v_mfma_f32_16x16x32_bf16 v[16:19], v[84:87], v[56:59], v[16:19]\n"
        "v_mfma_f32_16x16x32_bf16 v[20:23], v[84:87], v[60:63], v[20:23]\n"
        "s_barrier\n"
        "ds_read_b128 v[48:51], v236 offset:32768\n"
        "ds_read_b128 v[52:55], v236 offset:34816\n"
        "ds_read_b64_tr_b16 v[76:77], v238 offset:40960\n"
        "ds_read_b64_tr_b16 v[78:79], v238 offset:41472\n"
        "ds_read_b64_tr_b16 v[80:81], v238 offset:49152\n"
        "ds_read_b64_tr_b16 v[82:83], v238 offset:49664\n"
        "ds_read_b64_tr_b16 v[72:73], v238 offset:32768\n"
        "ds_read_b64_tr_b16 v[74:75], v238 offset:33280\n"
        "s_waitcnt lgkmcnt(8)\n"
        "s_nop 0\n"
        "v_mfma_f32_16x16x32_bf16 v[104:107], v[64:67], v[88:91], v[104:107]\n"
        "v_mfma_f32_16x16x32_bf16 v[108:111], v[68:71], v[88:91], v[108:111]\n"
        "v_mfma_f32_16x16x32_bf16 v[120:123], v[64:67], v[92:95], v[120:123]\n"
        "v_mfma_f32_16x16x32_bf16 v[124:127], v[68:71], v[92:95], v[124:127]\n"
        "v_mfma_f32_16x16x32_bf16 v[24:27], v[84:87], v[64:67], v[24:27]\n"
        "v_mfma_f32_16x16x32_bf16 v[28:31], v[84:87], v[68:71], v[28:31]\n"
        "ds_read_b128 v[56:59], v236 offset:36864\n"
        "ds_read_b128 v[60:63], v236 offset:38912\n"
        "s_waitcnt lgkmcnt(6)\n"
        "v_mfma_f32_16x16x32_bf16 v[96:99], v[48:51], v[76:79], v[96:99]\n"
        "v_mfma_f32_16x16x32_bf16 v[100:103], v[52:55], v[76:79], v[100:103]\n"
        "s_waitcnt lgkmcnt(4)\n"
        "v_mfma_f32_16x16x32_bf16 v[112:115], v[48:51], v[80:83], v[112:115]\n"
        "v_mfma_f32_16x16x32_bf16 v[116:119], v[52:55], v[80:83], v[116:119]\n"
        "s_waitcnt lgkmcnt(2)\n"
        "s_nop 0\n"
        "v_mfma_f32_16x16x32_bf16 v[16:19], v[72:75], v[48:51], v[16:19]\n"
        "v_mfma_f32_16x16x32_bf16 v[20:23], v[72:75], v[52:55], v[20:23]\n"
        "ds_read_b128 v[64:67], v237 offset:32768\n"
        "ds_read_b128 v[68:71], v237 offset:34816\n"
        "ds_read_b64_tr_b16 v[88:89], v238 offset:45056\n"
        "ds_read_b64_tr_b16 v[90:91], v238 offset:45568\n"
        "ds_read_b64_tr_b16 v[92:93], v238 offset:53248\n"
        "ds_read_b64_tr_b16 v[94:95], v238 offset:53760\n"
        "ds_read_b64_tr_b16 v[84:85], v238 offset:36864\n"
        "ds_read_b64_tr_b16 v[86:87], v238 offset:37376\n"
        "s_waitcnt lgkmcnt(8)\n"
        "s_nop 0\n"
        "v_mfma_f32_16x16x32_bf16 v[104:107], v[56:59], v[76:79], v[104:107]\n"
        "v_mfma_f32_16x16x32_bf16 v[108:111], v[60:63], v[76:79], v[108:111]\n"
        "v_mfma_f32_16x16x32_bf16 v[120:123], v[56:59], v[80:83], v[120:123]\n"
        "v_mfma_f32_16x16x32_bf16 v[124:127], v[60:63], v[80:83], v[124:127]\n"
        "v_mfma_f32_16x16x32_bf16 v[24:27], v[72:75], v[56:59], v[24:27]\n"
        "v_mfma_f32_16x16x32_bf16 v[28:31], v[72:75], v[60:63], v[28:31]\n"
        "ds_read_b128 v[48:51], v237 offset:36864\n"
        "ds_read_b128 v[52:55], v237 offset:38912\n"
        "s_waitcnt lgkmcnt(6)\n"
        "v_mfma_f32_16x16x32_bf16 v[96:99], v[64:67], v[88:91], v[96:99]\n"
        "v_mfma_f32_16x16x32_bf16 v[100:103], v[68:71], v[88:91], v[100:103]\n"
        "s_waitcnt lgkmcnt(4)\n"
        "v_mfma_f32_16x16x32_bf16 v[112:115], v[64:67], v[92:95], v[112:115]\n"
        "v_mfma_f32_16x16x32_bf16 v[116:119], v[68:71], v[92:95], v[116:119]\n"
        "s_waitcnt lgkmcnt(2)\n"
        "v_mfma_f32_16x16x32_bf16 v[16:19], v[84:87], v[64:67], v[16:19]\n"
        "v_mfma_f32_16x16x32_bf16 v[20:23], v[84:87], v[68:71], v[20:23]\n"
        "s_waitcnt vmcnt(0)\n"
        "s_barrier\n"
        "s_nop 0\n"
        "v_xor_b32 v236, 0x10000, v236\n"
        "v_xor_b32 v237, 0x10000, v237\n"
        "v_xor_b32 v238, 0x10000, v238\n"
        "ds_read_b128 v[56:59], v236 offset:0\n"
        "ds_read_b128 v[60:63], v236 offset:2048\n"
        "ds_read_b64_tr_b16 v[76:77], v238 offset:8192\n"
        "ds_read_b64_tr_b16 v[78:79], v238 offset:8704\n"
        "ds_read_b64_tr_b16 v[80:81], v238 offset:16384\n"
        "ds_read_b64_tr_b16 v[82:83], v238 offset:16896\n"
        "ds_read_b64_tr_b16 v[72:73], v238 offset:0\n"
        "ds_read_b64_tr_b16 v[74:75], v238 offset:512\n"
        "s_waitcnt lgkmcnt(8)\n"
        "s_nop 0\n"
        "v_mfma_f32_16x16x32_bf16 v[104:107], v[48:51], v[88:91], v[104:107]\n"
        "v_mfma_f32_16x16x32_bf16 v[108:111], v[52:55], v[88:91], v[108:111]\n"
        "v_mfma_f32_16x16x32_bf16 v[120:123], v[48:51], v[92:95], v[120:123]\n"
        "v_mfma_f32_16x16x32_bf16 v[124:127], v[52:55], v[92:95], v[124:127]\n"
        "v_mfma_f32_16x16x32_bf16 v[24:27], v[84:87], v[48:51], v[24:27]\n"
        "v_mfma_f32_16x16x32_bf16 v[28:31], v[84:87], v[52:55], v[28:31]\n"
        "ds_read_b128 v[64:67], v236 offset:4096\n"
        "ds_read_b128 v[68:71], v236 offset:6144\n"
        "s_waitcnt lgkmcnt(6)\n"
        "v_mfma_f32_16x16x32_bf16 v[96:99], v[56:59], v[76:79], v[96:99]\n"
        "v_mfma_f32_16x16x32_bf16 v[100:103], v[60:63], v[76:79], v[100:103]\n"
        "s_waitcnt lgkmcnt(4)\n"
        "v_mfma_f32_16x16x32_bf16 v[112:115], v[56:59], v[80:83], v[112:115]\n"
        "v_mfma_f32_16x16x32_bf16 v[116:119], v[60:63], v[80:83], v[116:119]\n"
        "s_waitcnt lgkmcnt(2)\n"
        "s_nop 0\n"
        "v_mfma_f32_16x16x32_bf16 v[16:19], v[72:75], v[56:59], v[16:19]\n"
        "v_mfma_f32_16x16x32_bf16 v[20:23], v[72:75], v[60:63], v[20:23]\n"
        "ds_read_b128 v[48:51], v237 offset:0\n"
        "ds_read_b128 v[52:55], v237 offset:2048\n"
        "ds_read_b64_tr_b16 v[88:89], v238 offset:12288\n"
        "ds_read_b64_tr_b16 v[90:91], v238 offset:12800\n"
        "ds_read_b64_tr_b16 v[92:93], v238 offset:20480\n"
        "ds_read_b64_tr_b16 v[94:95], v238 offset:20992\n"
        "ds_read_b64_tr_b16 v[84:85], v238 offset:4096\n"
        "ds_read_b64_tr_b16 v[86:87], v238 offset:4608\n"
        "s_waitcnt lgkmcnt(8)\n"
        "s_nop 0\n"
        "v_mfma_f32_16x16x32_bf16 v[104:107], v[64:67], v[76:79], v[104:107]\n"
        "v_mfma_f32_16x16x32_bf16 v[108:111], v[68:71], v[76:79], v[108:111]\n"
        "v_mfma_f32_16x16x32_bf16 v[120:123], v[64:67], v[80:83], v[120:123]\n"
        "v_mfma_f32_16x16x32_bf16 v[124:127], v[68:71], v[80:83], v[124:127]\n"
        "v_mfma_f32_16x16x32_bf16 v[24:27], v[72:75], v[64:67], v[24:27]\n"
        "v_mfma_f32_16x16x32_bf16 v[28:31], v[72:75], v[68:71], v[28:31]\n"
        "ds_read_b128 v[56:59], v237 offset:4096\n"
        "ds_read_b128 v[60:63], v237 offset:6144\n"
        "s_waitcnt lgkmcnt(6)\n"
        "v_mfma_f32_16x16x32_bf16 v[96:99], v[48:51], v[88:91], v[96:99]\n"
        "v_mfma_f32_16x16x32_bf16 v[100:103], v[52:55], v[88:91], v[100:103]\n"
        "s_waitcnt lgkmcnt(4)\n"
        "v_mfma_f32_16x16x32_bf16 v[112:115], v[48:51], v[92:95], v[112:115]\n"
        "v_mfma_f32_16x16x32_bf16 v[116:119], v[52:55], v[92:95], v[116:119]\n"
        "s_waitcnt lgkmcnt(2)\n"
        "v_mfma_f32_16x16x32_bf16 v[16:19], v[84:87], v[48:51], v[16:19]\n"
        "v_mfma_f32_16x16x32_bf16 v[20:23], v[84:87], v[52:55], v[20:23]\n"
        "s_barrier\n"
        "ds_read_b128 v[64:67], v236 offset:32768\n"
        "ds_read_b128 v[68:71], v236 offset:34816\n"
        "ds_read_b64_tr_b16 v[76:77], v238 offset:40960\n"
        "ds_read_b64_tr_b16 v[78:79], v238 offset:41472\n"
        "ds_read_b64_tr_b16 v[80:81], v238 offset:49152\n"
        "ds_read_b64_tr_b16 v[82:83], v238 offset:49664\n"
        "ds_read_b64_tr_b16 v[72:73], v238 offset:32768\n"
        "ds_read_b64_tr_b16 v[74:75], v238 offset:33280\n"
        "s_waitcnt lgkmcnt(8)\n"
        "s_nop 0\n"
        "v_mfma_f32_16x16x32_bf16 v[104:107], v[56:59], v[88:91], v[104:107]\n"
        "v_mfma_f32_16x16x32_bf16 v[108:111], v[60:63], v[88:91], v[108:111]\n"
        "v_mfma_f32_16x16x32_bf16 v[120:123], v[56:59], v[92:95], v[120:123]\n"
        "v_mfma_f32_16x16x32_bf16 v[124:127], v[60:63], v[92:95], v[124:127]\n"
        "v_mfma_f32_16x16x32_bf16 v[24:27], v[84:87], v[56:59], v[24:27]\n"
        "v_mfma_f32_16x16x32_bf16 v[28:31], v[84:87], v[60:63], v[28:31]\n"
        "ds_read_b128 v[48:51], v236 offset:36864\n"
        "ds_read_b128 v[52:55], v236 offset:38912\n"
        "s_waitcnt lgkmcnt(6)\n"
        "v_mfma_f32_16x16x32_bf16 v[96:99], v[64:67], v[76:79], v[96:99]\n"
        "v_mfma_f32_16x16x32_bf16 v[100:103], v[68:71], v[76:79], v[100:103]\n"
        "s_waitcnt lgkmcnt(4)\n"
        "v_mfma_f32_16x16x32_bf16 v[112:115], v[64:67], v[80:83], v[112:115]\n"
        "v_mfma_f32_16x16x32_bf16 v[116:119], v[68:71], v[80:83], v[116:119]\n"
        "s_waitcnt lgkmcnt(2)\n"
        "s_nop 0\n"
        "v_mfma_f32_16x16x32_bf16 v[16:19], v[72:75], v[64:67], v[16:19]\n"
        "v_mfma_f32_16x16x32_bf16 v[20:23], v[72:75], v[68:71], v[20:23]\n"
        "ds_read_b128 v[56:59], v237 offset:32768\n"
        "ds_read_b128 v[60:63], v237 offset:34816\n"
        "ds_read_b64_tr_b16 v[88:89], v238 offset:45056\n"
        "ds_read_b64_tr_b16 v[90:91], v238 offset:45568\n"
        "ds_read_b64_tr_b16 v[92:93], v238 offset:53248\n"
        "ds_read_b64_tr_b16 v[94:95], v238 offset:53760\n"
        "ds_read_b64_tr_b16 v[84:85], v238 offset:36864\n"
        "ds_read_b64_tr_b16 v[86:87], v238 offset:37376\n"
        "s_waitcnt lgkmcnt(8)\n"
        "s_nop 0\n"
        "v_mfma_f32_16x16x32_bf16 v[104:107], v[48:51], v[76:79], v[104:107]\n"
        "v_mfma_f32_16x16x32_bf16 v[108:111], v[52:55], v[76:79], v[108:111]\n"
        "v_mfma_f32_16x16x32_bf16 v[120:123], v[48:51], v[80:83], v[120:123]\n"
        "v_mfma_f32_16x16x32_bf16 v[124:127], v[52:55], v[80:83], v[124:127]\n"
        "v_mfma_f32_16x16x32_bf16 v[24:27], v[72:75], v[48:51], v[24:27]\n"
        "v_mfma_f32_16x16x32_bf16 v[28:31], v[72:75], v[52:55], v[28:31]\n"
        "ds_read_b128 v[64:67], v237 offset:36864\n"
        "ds_read_b128 v[68:71], v237 offset:38912\n"
        "s_waitcnt lgkmcnt(6)\n"
        "v_mfma_f32_16x16x32_bf16 v[96:99], v[56:59], v[88:91], v[96:99]\n"
        "v_mfma_f32_16x16x32_bf16 v[100:103], v[60:63], v[88:91], v[100:103]\n"
        "s_waitcnt lgkmcnt(4)\n"
        "v_mfma_f32_16x16x32_bf16 v[112:115], v[56:59], v[92:95], v[112:115]\n"
        "v_mfma_f32_16x16x32_bf16 v[116:119], v[60:63], v[92:95], v[116:119]\n"
        "s_waitcnt lgkmcnt(2)\n"
        "v_mfma_f32_16x16x32_bf16 v[16:19], v[84:87], v[56:59], v[16:19]\n"
        "v_mfma_f32_16x16x32_bf16 v[20:23], v[84:87], v[60:63], v[20:23]\n"
        "s_barrier\n"
        "v_xor_b32 v236, 0x10000, v236\n"
        "v_xor_b32 v237, 0x10000, v237\n"
        "v_xor_b32 v238, 0x10000, v238\n"
        "ds_read_b128 v[48:51], v236 offset:0\n"
        "ds_read_b128 v[52:55], v236 offset:2048\n"
        "ds_read_b64_tr_b16 v[76:77], v238 offset:8192\n"
        "ds_read_b64_tr_b16 v[78:79], v238 offset:8704\n"
        "ds_read_b64_tr_b16 v[80:81], v238 offset:16384\n"
        "ds_read_b64_tr_b16 v[82:83], v238 offset:16896\n"
        "ds_read_b64_tr_b16 v[72:73], v238 offset:0\n"
        "ds_read_b64_tr_b16 v[74:75], v238 offset:512\n"
        "s_waitcnt lgkmcnt(8)\n"
        "s_nop 0\n"
        "v_mfma_f32_16x16x32_bf16 v[104:107], v[64:67], v[88:91], v[104:107]\n"
        "v_mfma_f32_16x16x32_bf16 v[108:111], v[68:71], v[88:91], v[108:111]\n"
        "v_mfma_f32_16x16x32_bf16 v[120:123], v[64:67], v[92:95], v[120:123]\n"
        "v_mfma_f32_16x16x32_bf16 v[124:127], v[68:71], v[92:95], v[124:127]\n"
        "v_mfma_f32_16x16x32_bf16 v[24:27], v[84:87], v[64:67], v[24:27]\n"
        "v_mfma_f32_16x16x32_bf16 v[28:31], v[84:87], v[68:71], v[28:31]\n"
        "ds_read_b128 v[56:59], v236 offset:4096\n"
        "ds_read_b128 v[60:63], v236 offset:6144\n"
        "s_waitcnt lgkmcnt(6)\n"
        "v_mfma_f32_16x16x32_bf16 v[96:99], v[48:51], v[76:79], v[96:99]\n"
        "v_mfma_f32_16x16x32_bf16 v[100:103], v[52:55], v[76:79], v[100:103]\n"
        "s_waitcnt lgkmcnt(4)\n"
        "v_mfma_f32_16x16x32_bf16 v[112:115], v[48:51], v[80:83], v[112:115]\n"
        "v_mfma_f32_16x16x32_bf16 v[116:119], v[52:55], v[80:83], v[116:119]\n"
        "s_waitcnt lgkmcnt(2)\n"
        "s_nop 0\n"
        "v_mfma_f32_16x16x32_bf16 v[16:19], v[72:75], v[48:51], v[16:19]\n"
        "v_mfma_f32_16x16x32_bf16 v[20:23], v[72:75], v[52:55], v[20:23]\n"
        "ds_read_b128 v[64:67], v237 offset:0\n"
        "ds_read_b128 v[68:71], v237 offset:2048\n"
        "ds_read_b64_tr_b16 v[88:89], v238 offset:12288\n"
        "ds_read_b64_tr_b16 v[90:91], v238 offset:12800\n"
        "ds_read_b64_tr_b16 v[92:93], v238 offset:20480\n"
        "ds_read_b64_tr_b16 v[94:95], v238 offset:20992\n"
        "ds_read_b64_tr_b16 v[84:85], v238 offset:4096\n"
        "ds_read_b64_tr_b16 v[86:87], v238 offset:4608\n"
        "s_waitcnt lgkmcnt(8)\n"
        "s_nop 0\n"
        "v_mfma_f32_16x16x32_bf16 v[104:107], v[56:59], v[76:79], v[104:107]\n"
        "v_mfma_f32_16x16x32_bf16 v[108:111], v[60:63], v[76:79], v[108:111]\n"
        "v_mfma_f32_16x16x32_bf16 v[120:123], v[56:59], v[80:83], v[120:123]\n"
        "v_mfma_f32_16x16x32_bf16 v[124:127], v[60:63], v[80:83], v[124:127]\n"
        "v_mfma_f32_16x16x32_bf16 v[24:27], v[72:75], v[56:59], v[24:27]\n"
        "v_mfma_f32_16x16x32_bf16 v[28:31], v[72:75], v[60:63], v[28:31]\n"
        "ds_read_b128 v[48:51], v237 offset:4096\n"
        "ds_read_b128 v[52:55], v237 offset:6144\n"
        "s_waitcnt lgkmcnt(6)\n"
        "v_mfma_f32_16x16x32_bf16 v[96:99], v[64:67], v[88:91], v[96:99]\n"
        "v_mfma_f32_16x16x32_bf16 v[100:103], v[68:71], v[88:91], v[100:103]\n"
        "s_waitcnt lgkmcnt(4)\n"
        "v_mfma_f32_16x16x32_bf16 v[112:115], v[64:67], v[92:95], v[112:115]\n"
        "v_mfma_f32_16x16x32_bf16 v[116:119], v[68:71], v[92:95], v[116:119]\n"
        "s_waitcnt lgkmcnt(2)\n"
        "v_mfma_f32_16x16x32_bf16 v[16:19], v[84:87], v[64:67], v[16:19]\n"
        "v_mfma_f32_16x16x32_bf16 v[20:23], v[84:87], v[68:71], v[20:23]\n"
        "s_barrier\n"
        "ds_read_b128 v[56:59], v236 offset:32768\n"
        "ds_read_b128 v[60:63], v236 offset:34816\n"
        "ds_read_b64_tr_b16 v[76:77], v238 offset:40960\n"
        "ds_read_b64_tr_b16 v[78:79], v238 offset:41472\n"
        "ds_read_b64_tr_b16 v[80:81], v238 offset:49152\n"
        "ds_read_b64_tr_b16 v[82:83], v238 offset:49664\n"
        "ds_read_b64_tr_b16 v[72:73], v238 offset:32768\n"
        "ds_read_b64_tr_b16 v[74:75], v238 offset:33280\n"
        "s_waitcnt lgkmcnt(8)\n"
        "s_nop 0\n"
        "v_mfma_f32_16x16x32_bf16 v[104:107], v[48:51], v[88:91], v[104:107]\n"
        "v_mfma_f32_16x16x32_bf16 v[108:111], v[52:55], v[88:91], v[108:111]\n"
        "v_mfma_f32_16x16x32_bf16 v[120:123], v[48:51], v[92:95], v[120:123]\n"
        "v_mfma_f32_16x16x32_bf16 v[124:127], v[52:55], v[92:95], v[124:127]\n"
        "v_mfma_f32_16x16x32_bf16 v[24:27], v[84:87], v[48:51], v[24:27]\n"
        "v_mfma_f32_16x16x32_bf16 v[28:31], v[84:87], v[52:55], v[28:31]\n"
        "ds_read_b128 v[64:67], v236 offset:36864\n"
        "ds_read_b128 v[68:71], v236 offset:38912\n"
        "s_waitcnt lgkmcnt(6)\n"
        "v_mfma_f32_16x16x32_bf16 v[96:99], v[56:59], v[76:79], v[96:99]\n"
        "v_mfma_f32_16x16x32_bf16 v[100:103], v[60:63], v[76:79], v[100:103]\n"
        "s_waitcnt lgkmcnt(4)\n"
        "v_mfma_f32_16x16x32_bf16 v[112:115], v[56:59], v[80:83], v[112:115]\n"
        "v_mfma_f32_16x16x32_bf16 v[116:119], v[60:63], v[80:83], v[116:119]\n"
        "s_waitcnt lgkmcnt(2)\n"
        "s_nop 0\n"
        "v_mfma_f32_16x16x32_bf16 v[16:19], v[72:75], v[56:59], v[16:19]\n"
        "v_mfma_f32_16x16x32_bf16 v[20:23], v[72:75], v[60:63], v[20:23]\n"
        "ds_read_b128 v[48:51], v237 offset:32768\n"
        "ds_read_b128 v[52:55], v237 offset:34816\n"
        "ds_read_b64_tr_b16 v[88:89], v238 offset:45056\n"
        "ds_read_b64_tr_b16 v[90:91], v238 offset:45568\n"
        "ds_read_b64_tr_b16 v[92:93], v238 offset:53248\n"
        "ds_read_b64_tr_b16 v[94:95], v238 offset:53760\n"
        "ds_read_b64_tr_b16 v[84:85], v238 offset:36864\n"
        "ds_read_b64_tr_b16 v[86:87], v238 offset:37376\n"
        "s_waitcnt lgkmcnt(8)\n"
        "s_nop 0\n"
        "v_mfma_f32_16x16x32_bf16 v[104:107], v[64:67], v[76:79], v[104:107]\n"
        "v_mfma_f32_16x16x32_bf16 v[108:111], v[68:71], v[76:79], v[108:111]\n"
        "v_mfma_f32_16x16x32_bf16 v[120:123], v[64:67], v[80:83], v[120:123]\n"
        "v_mfma_f32_16x16x32_bf16 v[124:127], v[68:71], v[80:83], v[124:127]\n"
        "v_mfma_f32_16x16x32_bf16 v[24:27], v[72:75], v[64:67], v[24:27]\n"
        "v_mfma_f32_16x16x32_bf16 v[28:31], v[72:75], v[68:71], v[28:31]\n"
        "ds_read_b128 v[56:59], v237 offset:36864\n"
        "ds_read_b128 v[60:63], v237 offset:38912\n"
        "s_waitcnt lgkmcnt(6)\n"
        "v_mfma_f32_16x16x32_bf16 v[96:99], v[48:51], v[88:91], v[96:99]\n"
        "v_mfma_f32_16x16x32_bf16 v[100:103], v[52:55], v[88:91], v[100:103]\n"
        "s_waitcnt lgkmcnt(4)\n"
        "v_mfma_f32_16x16x32_bf16 v[112:115], v[48:51], v[92:95], v[112:115]\n"
        "v_mfma_f32_16x16x32_bf16 v[116:119], v[52:55], v[92:95], v[116:119]\n"
        "s_waitcnt lgkmcnt(2)\n"
        "v_mfma_f32_16x16x32_bf16 v[16:19], v[84:87], v[48:51], v[16:19]\n"
        "v_mfma_f32_16x16x32_bf16 v[20:23], v[84:87], v[52:55], v[20:23]\n"
        "s_barrier\n"
        "v_xor_b32 v236, 0x10000, v236\n"
        "v_xor_b32 v237, 0x10000, v237\n"
        "v_xor_b32 v238, 0x10000, v238\n"
        "ds_read_b128 v[64:67], v236 offset:0\n"
        "ds_read_b128 v[68:71], v236 offset:2048\n"
        "ds_read_b64_tr_b16 v[76:77], v238 offset:8192\n"
        "ds_read_b64_tr_b16 v[78:79], v238 offset:8704\n"
        "ds_read_b64_tr_b16 v[80:81], v238 offset:16384\n"
        "ds_read_b64_tr_b16 v[82:83], v238 offset:16896\n"
        "ds_read_b64_tr_b16 v[72:73], v238 offset:0\n"
        "ds_read_b64_tr_b16 v[74:75], v238 offset:512\n"
        "s_waitcnt lgkmcnt(8)\n"
        "s_nop 0\n"
        "v_mfma_f32_16x16x32_bf16 v[104:107], v[56:59], v[88:91], v[104:107]\n"
        "v_mfma_f32_16x16x32_bf16 v[108:111], v[60:63], v[88:91], v[108:111]\n"
        "v_mfma_f32_16x16x32_bf16 v[120:123], v[56:59], v[92:95], v[120:123]\n"
        "v_mfma_f32_16x16x32_bf16 v[124:127], v[60:63], v[92:95], v[124:127]\n"
        "v_mfma_f32_16x16x32_bf16 v[24:27], v[84:87], v[56:59], v[24:27]\n"
        "v_mfma_f32_16x16x32_bf16 v[28:31], v[84:87], v[60:63], v[28:31]\n"
        "ds_read_b128 v[48:51], v236 offset:4096\n"
        "ds_read_b128 v[52:55], v236 offset:6144\n"
        "s_waitcnt lgkmcnt(6)\n"
        "v_mfma_f32_16x16x32_bf16 v[96:99], v[64:67], v[76:79], v[96:99]\n"
        "v_mfma_f32_16x16x32_bf16 v[100:103], v[68:71], v[76:79], v[100:103]\n"
        "s_waitcnt lgkmcnt(4)\n"
        "v_mfma_f32_16x16x32_bf16 v[112:115], v[64:67], v[80:83], v[112:115]\n"
        "v_mfma_f32_16x16x32_bf16 v[116:119], v[68:71], v[80:83], v[116:119]\n"
        "s_waitcnt lgkmcnt(2)\n"
        "s_nop 0\n"
        "v_mfma_f32_16x16x32_bf16 v[16:19], v[72:75], v[64:67], v[16:19]\n"
        "v_mfma_f32_16x16x32_bf16 v[20:23], v[72:75], v[68:71], v[20:23]\n"
        "ds_read_b128 v[56:59], v237 offset:0\n"
        "ds_read_b128 v[60:63], v237 offset:2048\n"
        "ds_read_b64_tr_b16 v[88:89], v238 offset:12288\n"
        "ds_read_b64_tr_b16 v[90:91], v238 offset:12800\n"
        "ds_read_b64_tr_b16 v[92:93], v238 offset:20480\n"
        "ds_read_b64_tr_b16 v[94:95], v238 offset:20992\n"
        "ds_read_b64_tr_b16 v[84:85], v238 offset:4096\n"
        "ds_read_b64_tr_b16 v[86:87], v238 offset:4608\n"
        "s_waitcnt lgkmcnt(8)\n"
        "s_nop 0\n"
        "v_mfma_f32_16x16x32_bf16 v[104:107], v[48:51], v[76:79], v[104:107]\n"
        "v_mfma_f32_16x16x32_bf16 v[108:111], v[52:55], v[76:79], v[108:111]\n"
        "v_mfma_f32_16x16x32_bf16 v[120:123], v[48:51], v[80:83], v[120:123]\n"
        "v_mfma_f32_16x16x32_bf16 v[124:127], v[52:55], v[80:83], v[124:127]\n"
        "v_mfma_f32_16x16x32_bf16 v[24:27], v[72:75], v[48:51], v[24:27]\n"
        "v_mfma_f32_16x16x32_bf16 v[28:31], v[72:75], v[52:55], v[28:31]\n"
        "ds_read_b128 v[64:67], v237 offset:4096\n"
        "ds_read_b128 v[68:71], v237 offset:6144\n"
        "s_waitcnt lgkmcnt(6)\n"
        "v_mfma_f32_16x16x32_bf16 v[96:99], v[56:59], v[88:91], v[96:99]\n"
        "v_mfma_f32_16x16x32_bf16 v[100:103], v[60:63], v[88:91], v[100:103]\n"
        "s_waitcnt lgkmcnt(4)\n"
        "v_mfma_f32_16x16x32_bf16 v[112:115], v[56:59], v[92:95], v[112:115]\n"
        "v_mfma_f32_16x16x32_bf16 v[116:119], v[60:63], v[92:95], v[116:119]\n"
        "s_waitcnt lgkmcnt(2)\n"
        "v_mfma_f32_16x16x32_bf16 v[16:19], v[84:87], v[56:59], v[16:19]\n"
        "v_mfma_f32_16x16x32_bf16 v[20:23], v[84:87], v[60:63], v[20:23]\n"
        "s_barrier\n"
        "ds_read_b128 v[48:51], v236 offset:32768\n"
        "ds_read_b128 v[52:55], v236 offset:34816\n"
        "ds_read_b64_tr_b16 v[76:77], v238 offset:40960\n"
        "ds_read_b64_tr_b16 v[78:79], v238 offset:41472\n"
        "ds_read_b64_tr_b16 v[80:81], v238 offset:49152\n"
        "ds_read_b64_tr_b16 v[82:83], v238 offset:49664\n"
        "ds_read_b64_tr_b16 v[72:73], v238 offset:32768\n"
        "ds_read_b64_tr_b16 v[74:75], v238 offset:33280\n"
        "s_waitcnt lgkmcnt(8)\n"
        "s_nop 0\n"
        "v_mfma_f32_16x16x32_bf16 v[104:107], v[64:67], v[88:91], v[104:107]\n"
        "v_mfma_f32_16x16x32_bf16 v[108:111], v[68:71], v[88:91], v[108:111]\n"
        "v_mfma_f32_16x16x32_bf16 v[120:123], v[64:67], v[92:95], v[120:123]\n"
        "v_mfma_f32_16x16x32_bf16 v[124:127], v[68:71], v[92:95], v[124:127]\n"
        "v_mfma_f32_16x16x32_bf16 v[24:27], v[84:87], v[64:67], v[24:27]\n"
        "v_mfma_f32_16x16x32_bf16 v[28:31], v[84:87], v[68:71], v[28:31]\n"
        "ds_read_b128 v[56:59], v236 offset:36864\n"
        "ds_read_b128 v[60:63], v236 offset:38912\n"
        "s_waitcnt lgkmcnt(6)\n"
        "v_mfma_f32_16x16x32_bf16 v[96:99], v[48:51], v[76:79], v[96:99]\n"
        "v_mfma_f32_16x16x32_bf16 v[100:103], v[52:55], v[76:79], v[100:103]\n"
        "s_waitcnt lgkmcnt(4)\n"
        "v_mfma_f32_16x16x32_bf16 v[112:115], v[48:51], v[80:83], v[112:115]\n"
        "v_mfma_f32_16x16x32_bf16 v[116:119], v[52:55], v[80:83], v[116:119]\n"
        "s_waitcnt lgkmcnt(2)\n"
        "s_nop 0\n"
        "v_mfma_f32_16x16x32_bf16 v[16:19], v[72:75], v[48:51], v[16:19]\n"
        "v_mfma_f32_16x16x32_bf16 v[20:23], v[72:75], v[52:55], v[20:23]\n"
        "ds_read_b128 v[64:67], v237 offset:32768\n"
        "ds_read_b128 v[68:71], v237 offset:34816\n"
        "ds_read_b64_tr_b16 v[88:89], v238 offset:45056\n"
        "ds_read_b64_tr_b16 v[90:91], v238 offset:45568\n"
        "ds_read_b64_tr_b16 v[92:93], v238 offset:53248\n"
        "ds_read_b64_tr_b16 v[94:95], v238 offset:53760\n"
        "ds_read_b64_tr_b16 v[84:85], v238 offset:36864\n"
        "ds_read_b64_tr_b16 v[86:87], v238 offset:37376\n"
        "s_waitcnt lgkmcnt(8)\n"
        "s_nop 0\n"
        "v_mfma_f32_16x16x32_bf16 v[104:107], v[56:59], v[76:79], v[104:107]\n"
        "v_mfma_f32_16x16x32_bf16 v[108:111], v[60:63], v[76:79], v[108:111]\n"
        "v_mfma_f32_16x16x32_bf16 v[120:123], v[56:59], v[80:83], v[120:123]\n"
        "v_mfma_f32_16x16x32_bf16 v[124:127], v[60:63], v[80:83], v[124:127]\n"
        "v_mfma_f32_16x16x32_bf16 v[24:27], v[72:75], v[56:59], v[24:27]\n"
        "v_mfma_f32_16x16x32_bf16 v[28:31], v[72:75], v[60:63], v[28:31]\n"
        "ds_read_b128 v[48:51], v237 offset:36864\n"
        "ds_read_b128 v[52:55], v237 offset:38912\n"
        "s_waitcnt lgkmcnt(6)\n"
        "v_mfma_f32_16x16x32_bf16 v[96:99], v[64:67], v[88:91], v[96:99]\n"
        "v_mfma_f32_16x16x32_bf16 v[100:103], v[68:71], v[88:91], v[100:103]\n"
        "s_waitcnt lgkmcnt(4)\n"
        "v_mfma_f32_16x16x32_bf16 v[112:115], v[64:67], v[92:95], v[112:115]\n"
        "v_mfma_f32_16x16x32_bf16 v[116:119], v[68:71], v[92:95], v[116:119]\n"
        "s_waitcnt lgkmcnt(2)\n"
        "v_mfma_f32_16x16x32_bf16 v[16:19], v[84:87], v[64:67], v[16:19]\n"
        "v_mfma_f32_16x16x32_bf16 v[20:23], v[84:87], v[68:71], v[20:23]\n"
        "s_waitcnt lgkmcnt(0)\n"
        "v_mfma_f32_16x16x32_bf16 v[104:107], v[48:51], v[88:91], v[104:107]\n"
        "v_mfma_f32_16x16x32_bf16 v[108:111], v[52:55], v[88:91], v[108:111]\n"
        "v_mfma_f32_16x16x32_bf16 v[120:123], v[48:51], v[92:95], v[120:123]\n"
        "v_mfma_f32_16x16x32_bf16 v[124:127], v[52:55], v[92:95], v[124:127]\n"
        "v_mfma_f32_16x16x32_bf16 v[24:27], v[84:87], v[48:51], v[24:27]\n"
        "v_mfma_f32_16x16x32_bf16 v[28:31], v[84:87], v[52:55], v[28:31]\n"
        "s_barrier\n"
        "s_nop 0\n"
        "s_mov_b32 s68, 0x3fb8aa3b\n"
        "s_mov_b32 s69, 0x3fb8aa3b\n"
        "s_mov_b32 s70, 0x358637bd\n"
        "s_mov_b32 s71, 0x358637bd\n"
        "v_pk_mul_f32 v[48:49], v[96:97], s[68:69]\n"
        "v_pk_mul_f32 v[50:51], v[98:99], s[68:69]\n"
        "v_pk_mul_f32 v[52:53], v[100:101], s[68:69]\n"
        "v_pk_mul_f32 v[54:55], v[102:103], s[68:69]\n"
        "v_pk_mul_f32 v[56:57], v[104:105], s[68:69]\n"
        "v_pk_mul_f32 v[58:59], v[106:107], s[68:69]\n"
        "v_pk_mul_f32 v[60:61], v[108:109], s[68:69]\n"
        "v_pk_mul_f32 v[62:63], v[110:111], s[68:69]\n"
        "buffer_load_dwordx4 v[128:131], v11, s[52:55], s74 offen offset:0 sc1\n"
        "v_exp_f32 v48, v48 clamp\n"
        "v_exp_f32 v49, v49 clamp\n"
        "v_exp_f32 v50, v50 clamp\n"
        "v_exp_f32 v51, v51 clamp\n"
        "v_exp_f32 v52, v52 clamp\n"
        "v_exp_f32 v53, v53 clamp\n"
        "v_exp_f32 v54, v54 clamp\n"
        "v_exp_f32 v55, v55 clamp\n"
        "s_mov_b64 exec, s[66:67]\n"
        "buffer_load_dwordx4 v[132:135], v12, s[52:55], s74 offen offset:0 sc1\n"
        "s_mov_b64 exec, -1\n"
        "v_exp_f32 v56, v56 clamp\n"
        "v_exp_f32 v57, v57 clamp\n"
        "v_exp_f32 v58, v58 clamp\n"
        "v_exp_f32 v59, v59 clamp\n"
        "v_exp_f32 v60, v60 clamp\n"
        "v_exp_f32 v61, v61 clamp\n"
        "v_exp_f32 v62, v62 clamp\n"
        "v_exp_f32 v63, v63 clamp\n"
        "s_mov_b64 exec, s[66:67]\n"
        "buffer_load_dwordx4 v[136:139], v12, s[52:55], s74 offen offset:512 sc1\n"
        "s_mov_b64 exec, -1\n"
        "v_add_f32 v96, v96, |v96|\n"
        "v_add_f32 v97, v97, |v97|\n"
        "v_add_f32 v98, v98, |v98|\n"
        "v_add_f32 v99, v99, |v99|\n"
        "v_add_f32 v100, v100, |v100|\n"
        "v_add_f32 v101, v101, |v101|\n"
        "v_add_f32 v102, v102, |v102|\n"
        "v_add_f32 v103, v103, |v103|\n"
        "buffer_load_dwordx4 v[140:143], v11, s[52:55], s74 offen offset:2048 sc1\n"
        "v_add_f32 v104, v104, |v104|\n"
        "v_add_f32 v105, v105, |v105|\n"
        "v_add_f32 v106, v106, |v106|\n"
        "v_add_f32 v107, v107, |v107|\n"
        "v_add_f32 v108, v108, |v108|\n"
        "v_add_f32 v109, v109, |v109|\n"
        "v_add_f32 v110, v110, |v110|\n"
        "v_add_f32 v111, v111, |v111|\n"
        "s_mov_b64 exec, s[66:67]\n"
        "buffer_load_dwordx4 v[144:147], v12, s[52:55], s74 offen offset:2048 sc1\n"
        "s_mov_b64 exec, -1\n"
        "v_pk_fma_f32 v[96:97], v[96:97], 0.5, v[48:49] op_sel_hi:[1,0,1]\n"
        "v_pk_fma_f32 v[98:99], v[98:99], 0.5, v[50:51] op_sel_hi:[1,0,1]\n"
        "v_pk_fma_f32 v[100:101], v[100:101], 0.5, v[52:53] op_sel_hi:[1,0,1]\n"
        "v_pk_fma_f32 v[102:103], v[102:103], 0.5, v[54:55] op_sel_hi:[1,0,1]\n"
        "v_pk_fma_f32 v[104:105], v[104:105], 0.5, v[56:57] op_sel_hi:[1,0,1]\n"
        "v_pk_fma_f32 v[106:107], v[106:107], 0.5, v[58:59] op_sel_hi:[1,0,1]\n"
        "v_pk_fma_f32 v[108:109], v[108:109], 0.5, v[60:61] op_sel_hi:[1,0,1]\n"
        "v_pk_fma_f32 v[110:111], v[110:111], 0.5, v[62:63] op_sel_hi:[1,0,1]\n"
        "s_mov_b64 exec, s[66:67]\n"
        "buffer_load_dwordx4 v[148:151], v12, s[52:55], s74 offen offset:2560 sc1\n"
        "s_mov_b64 exec, -1\n"
        "v_pk_add_f32 v[96:97], v[96:97], s[70:71]\n"
        "v_pk_add_f32 v[98:99], v[98:99], s[70:71]\n"
        "v_pk_add_f32 v[100:101], v[100:101], s[70:71]\n"
        "v_pk_add_f32 v[102:103], v[102:103], s[70:71]\n"
        "v_pk_add_f32 v[104:105], v[104:105], s[70:71]\n"
        "v_pk_add_f32 v[106:107], v[106:107], s[70:71]\n"
        "v_pk_add_f32 v[108:109], v[108:109], s[70:71]\n"
        "v_pk_add_f32 v[110:111], v[110:111], s[70:71]\n"
        "buffer_load_dwordx4 v[152:155], v11, s[52:55], s75 offen offset:0 sc1\n"
        "v_cvt_pk_bf16_f32 v112, v112, v113\n"
        "v_cvt_pk_bf16_f32 v113, v114, v115\n"
        "v_cvt_pk_bf16_f32 v114, v116, v117\n"
        "v_cvt_pk_bf16_f32 v115, v118, v119\n"
        "v_cvt_pk_bf16_f32 v116, v120, v121\n"
        "v_cvt_pk_bf16_f32 v117, v122, v123\n"
        "v_cvt_pk_bf16_f32 v118, v124, v125\n"
        "v_cvt_pk_bf16_f32 v119, v126, v127\n"
        "v_cvt_pk_bf16_f32 v96, v96, v97\n"
        "v_cvt_pk_bf16_f32 v97, v98, v99\n"
        "v_cvt_pk_bf16_f32 v98, v100, v101\n"
        "v_cvt_pk_bf16_f32 v99, v102, v103\n"
        "v_cvt_pk_bf16_f32 v100, v104, v105\n"
        "v_cvt_pk_bf16_f32 v101, v106, v107\n"
        "v_cvt_pk_bf16_f32 v102, v108, v109\n"
        "v_cvt_pk_bf16_f32 v103, v110, v111\n"
        "v_mfma_f32_16x16x32_bf16 v[64:67], v[96:99], v[112:115], 0\n"
        "v_mfma_f32_16x16x32_bf16 v[68:71], v[96:99], v[232:235], 0\n"
        "v_mfma_f32_16x16x32_bf16 v[64:67], v[100:103], v[116:119], v[64:67]\n"
        "v_mfma_f32_16x16x32_bf16 v[68:71], v[100:103], v[232:235], v[68:71]\n"
        "s_mov_b32 s68, 0x3fb8aa3b\n"
        "s_mov_b32 s69, 0x3fb8aa3b\n"
        "s_mov_b32 s70, 0x358637bd\n"
        "s_mov_b32 s71, 0x358637bd\n"
        "v_pk_mul_f32 v[48:49], v[16:17], s[68:69]\n"
        "v_pk_mul_f32 v[50:51], v[18:19], s[68:69]\n"
        "v_pk_mul_f32 v[52:53], v[20:21], s[68:69]\n"
        "v_pk_mul_f32 v[54:55], v[22:23], s[68:69]\n"
        "v_pk_mul_f32 v[56:57], v[24:25], s[68:69]\n"
        "v_pk_mul_f32 v[58:59], v[26:27], s[68:69]\n"
        "v_pk_mul_f32 v[60:61], v[28:29], s[68:69]\n"
        "v_pk_mul_f32 v[62:63], v[30:31], s[68:69]\n"
        "s_mov_b64 exec, s[66:67]\n"
        "buffer_load_dwordx4 v[156:159], v12, s[52:55], s75 offen offset:0 sc1\n"
        "s_mov_b64 exec, -1\n"
        "v_exp_f32 v48, v48 clamp\n"
        "v_exp_f32 v49, v49 clamp\n"
        "v_exp_f32 v50, v50 clamp\n"
        "v_exp_f32 v51, v51 clamp\n"
        "v_exp_f32 v52, v52 clamp\n"
        "v_exp_f32 v53, v53 clamp\n"
        "v_exp_f32 v54, v54 clamp\n"
        "v_exp_f32 v55, v55 clamp\n"
        "s_mov_b64 exec, s[66:67]\n"
        "buffer_load_dwordx4 v[160:163], v12, s[52:55], s75 offen offset:512 sc1\n"
        "s_mov_b64 exec, -1\n"
        "v_exp_f32 v56, v56 clamp\n"
        "v_exp_f32 v57, v57 clamp\n"
        "v_exp_f32 v58, v58 clamp\n"
        "v_exp_f32 v59, v59 clamp\n"
        "v_exp_f32 v60, v60 clamp\n"
        "v_exp_f32 v61, v61 clamp\n"
        "v_exp_f32 v62, v62 clamp\n"
        "v_exp_f32 v63, v63 clamp\n"
        "buffer_load_dwordx4 v[164:167], v11, s[52:55], s75 offen offset:2048 sc1\n"
        "v_add_f32 v16, v16, |v16|\n"
        "v_add_f32 v17, v17, |v17|\n"
        "v_add_f32 v18, v18, |v18|\n"
        "v_add_f32 v19, v19, |v19|\n"
        "v_add_f32 v20, v20, |v20|\n"
        "v_add_f32 v21, v21, |v21|\n"
        "v_add_f32 v22, v22, |v22|\n"
        "v_add_f32 v23, v23, |v23|\n"
        "s_mov_b64 exec, s[66:67]\n"
        "buffer_load_dwordx4 v[168:171], v12, s[52:55], s75 offen offset:2048 sc1\n"
        "s_mov_b64 exec, -1\n"
        "v_add_f32 v24, v24, |v24|\n"
        "v_add_f32 v25, v25, |v25|\n"
        "v_add_f32 v26, v26, |v26|\n"
        "v_add_f32 v27, v27, |v27|\n"
        "v_add_f32 v28, v28, |v28|\n"
        "v_add_f32 v29, v29, |v29|\n"
        "v_add_f32 v30, v30, |v30|\n"
        "v_add_f32 v31, v31, |v31|\n"
        "v_pk_fma_f32 v[16:17], v[16:17], 0.5, v[48:49] op_sel_hi:[1,0,1]\n"
        "v_pk_fma_f32 v[18:19], v[18:19], 0.5, v[50:51] op_sel_hi:[1,0,1]\n"
        "v_pk_fma_f32 v[20:21], v[20:21], 0.5, v[52:53] op_sel_hi:[1,0,1]\n"
        "v_pk_fma_f32 v[22:23], v[22:23], 0.5, v[54:55] op_sel_hi:[1,0,1]\n"
        "v_pk_fma_f32 v[24:25], v[24:25], 0.5, v[56:57] op_sel_hi:[1,0,1]\n"
        "v_pk_fma_f32 v[26:27], v[26:27], 0.5, v[58:59] op_sel_hi:[1,0,1]\n"
        "v_pk_fma_f32 v[28:29], v[28:29], 0.5, v[60:61] op_sel_hi:[1,0,1]\n"
        "v_pk_fma_f32 v[30:31], v[30:31], 0.5, v[62:63] op_sel_hi:[1,0,1]\n"
        "v_pk_add_f32 v[16:17], v[16:17], s[70:71]\n"
        "v_pk_add_f32 v[18:19], v[18:19], s[70:71]\n"
        "v_pk_add_f32 v[20:21], v[20:21], s[70:71]\n"
        "v_pk_add_f32 v[22:23], v[22:23], s[70:71]\n"
        "v_pk_add_f32 v[24:25], v[24:25], s[70:71]\n"
        "v_pk_add_f32 v[26:27], v[26:27], s[70:71]\n"
        "v_pk_add_f32 v[28:29], v[28:29], s[70:71]\n"
        "v_pk_add_f32 v[30:31], v[30:31], s[70:71]\n"
        "s_mov_b32 s70, 0x00010001\n"
        "s_mov_b32 s71, 0x00010001\n"
        "buffer_store_dwordx4 v[64:67], v242, s[52:55], s6 offen sc1\n"
        "s_mov_b64 exec, s[70:71]\n"
        "buffer_store_dwordx4 v[68:71], v243, s[52:55], s7 offen sc1\n"
        "s_mov_b64 exec, -1\n"
        "v_cvt_pk_bf16_f32 v16, v16, v17\n"
        "v_cvt_pk_bf16_f32 v17, v18, v19\n"
        "v_cvt_pk_bf16_f32 v18, v20, v21\n"
        "v_cvt_pk_bf16_f32 v19, v22, v23\n"
        "v_cvt_pk_bf16_f32 v20, v24, v25\n"
        "v_cvt_pk_bf16_f32 v21, v26, v27\n"
        "v_cvt_pk_bf16_f32 v22, v28, v29\n"
        "v_cvt_pk_bf16_f32 v23, v30, v31\n"
        "v_permlane16_swap_b32 v16, v18\n"
        "v_permlane16_swap_b32 v17, v19\n"
        "v_permlane16_swap_b32 v20, v22\n"
        "v_permlane16_swap_b32 v21, v23\n"
        "ds_write_b128 v226, v[16:19] offset:8192\n"
        "ds_write_b128 v227, v[20:23] offset:8192\n"
        "s_waitcnt lgkmcnt(0)\n"
        "s_mov_b64 vcc, 0\n"
        "L_ATT_%=:\n"
        "s_setprio 0\n"
        "s_lshl_b32 s33, s60, 10\n"
        "v_mov_b32 v40, 0\n"
        "v_mov_b32 v41, 0\n"
        "v_mov_b32 v42, 0\n"
        "v_mov_b32 v43, 0\n"
        "v_mov_b32 v44, 0\n"
        "v_mov_b32 v45, 0\n"
        "v_mov_b32_e64 v46, 0\n"
        "v_mov_b32 v47, 0\n"
        "s_waitcnt vmcnt(10)\n"
        "v_mfma_f32_32x32x16_bf16 v[16:31], v[128:131], v[176:179], 0\n"
        "s_nop 11\n"
        "s_add_u32 s74, s78, 0x2000\n"
        "s_and_b32 s74, s74, 0xffff\n"
        "v_exp_f32 v16, v16\n"
        "v_exp_f32_e64 v17, v17\n"
        "v_smfmac_f32_32x32x32_bf16 v[200:215], v[172:175], v[40:47], v10\n"
        "s_mov_b64 exec, s[66:67]\n"
        "buffer_load_dwordx4 v[172:175], v12, s[52:55], s75 offen offset:2560 sc1\n"
        "s_mov_b64 exec, -1\n"
        "v_exp_f32 v18, v18\n"
        "v_exp_f32 v19, v19\n"
        "v_exp_f32 v20, v20\n"
        "v_exp_f32 v21, v21\n"
        "v_mfma_f32_32x32x16_bf16 v[32:47], v[128:131], v[180:183], 0\n"
        "buffer_load_dwordx4 v[128:131], v11, s[52:55], s74 offen offset:0 sc1\n"
        "v_exp_f32 v22, v22\n"
        "v_exp_f32 v23, v23\n"
        "v_mov_b32 v48, -1\n"
        "v_exp_f32 v24, v24\n"
        "v_mov_b32 v49, -1\n"
        "v_exp_f32 v25, v25\n"
        "v_mov_b32_e64 v50, -1\n"
        "v_exp_f32 v26, v26\n"
        "s_waitcnt vmcnt(11)\n"
        "v_smfmac_f32_32x32x32_bf16 v[184:199], v[132:135], v[16:23], v10\n"
        "v_mov_b32 v51, -1\n"
        "v_exp_f32 v27, v27\n"
        "v_exp_f32 v28, v28\n"
        "v_exp_f32 v29, v29\n"
        "v_exp_f32 v30, v30\n"
        "v_exp_f32 v31, v31\n"
        "v_exp_f32_e64 v32, v32\n"
        "v_exp_f32 v33, v33\n"
        "s_waitcnt vmcnt(10)\n"
        "v_smfmac_f32_32x32x32_bf16 v[184:199], v[136:139], v[24:31], v10\n"
        "v_exp_f32 v34, v34\n"
        "v_exp_f32 v35, v35\n"
        "s_cmp_lt_u32 s60, 2\n"
        "s_cbranch_scc1 L_red0_%=\n"
        "L_red0_b_%=:\n"
        "v_exp_f32_e64 v36, v36\n"
        "v_exp_f32 v37, v37\n"
        "s_waitcnt vmcnt(9)\n"
        "v_mfma_f32_32x32x16_bf16 v[16:31], v[140:143], v[176:179], 0\n"
        "v_exp_f32 v38, v38\n"
        "v_exp_f32 v39, v39\n"
        "v_exp_f32 v40, v40\n"
        "v_exp_f32 v41, v41\n"
        "v_exp_f32_e64 v42, v42\n"
        "v_smfmac_f32_32x32x32_bf16 v[200:215], v[132:135], v[32:39], v10\n"
        "s_mov_b64 exec, s[66:67]\n"
        "buffer_load_dwordx4 v[132:135], v12, s[52:55], s74 offen offset:0 sc1\n"
        "s_mov_b64 exec, -1\n"
        "v_exp_f32 v43, v43\n"
        "v_exp_f32 v44, v44\n"
        "v_exp_f32 v45, v45\n"
        "v_exp_f32 v46, v46\n"
        "v_exp_f32 v47, v47\n"
        "v_exp_f32 v16, v16\n"
        "v_exp_f32_e64 v17, v17\n"
        "v_smfmac_f32_32x32x32_bf16 v[200:215], v[136:139], v[40:47], v10\n"
        "s_mov_b64 exec, s[66:67]\n"
        "buffer_load_dwordx4 v[136:139], v12, s[52:55], s74 offen offset:512 sc1\n"
        "s_mov_b64 exec, -1\n"
        "v_exp_f32 v18, v18\n"
        "v_exp_f32 v19, v19\n"
        "v_exp_f32 v20, v20\n"
        "v_exp_f32 v21, v21\n"
        "v_mfma_f32_32x32x16_bf16 v[32:47], v[140:143], v[180:183], 0\n"
        "buffer_load_dwordx4 v[140:143], v11, s[52:55], s74 offen offset:2048 sc1\n"
        "v_exp_f32 v22, v22\n"
        "v_exp_f32 v23, v23\n"
        "v_exp_f32 v24, v24\n"
        "v_exp_f32 v25, v25\n"
        "v_exp_f32 v26, v26\n"
        "s_waitcnt vmcnt(11)\n"
        "v_smfmac_f32_32x32x32_bf16 v[184:199], v[144:147], v[16:23], v10\n"
        "v_exp_f32 v27, v27\n"
        "v_exp_f32 v28, v28\n"
        "v_exp_f32 v29, v29\n"
        "v_exp_f32 v30, v30\n"
        "v_exp_f32 v31, v31\n"
        "v_exp_f32 v32, v32\n"
        "v_exp_f32 v33, v33\n"
        "s_waitcnt vmcnt(10)\n"
        "v_smfmac_f32_32x32x32_bf16 v[184:199], v[148:151], v[24:31], v10\n"
        "v_exp_f32 v34, v34\n"
        "v_exp_f32 v35, v35\n"
        "v_exp_f32_e64 v36, v36\n"
        "v_exp_f32 v37, v37\n"
        "s_waitcnt vmcnt(9)\n"
        "v_mfma_f32_32x32x16_bf16 v[16:31], v[152:155], v[176:179], 0\n"
        "v_exp_f32 v38, v38\n"
        "v_exp_f32 v39, v39\n"
        "v_exp_f32 v40, v40\n"
        "v_exp_f32 v41, v41\n"
        "v_exp_f32_e64 v42, v42\n"
        "v_smfmac_f32_32x32x32_bf16 v[200:215], v[144:147], v[32:39], v10\n"
        "s_mov_b64 exec, s[66:67]\n"
        "buffer_load_dwordx4 v[144:147], v12, s[52:55], s74 offen offset:2048 sc1\n"
        "s_mov_b64 exec, -1\n"
        "v_exp_f32 v43, v43\n"
        "v_exp_f32 v44, v44\n"
        "v_exp_f32 v45, v45\n"
        "v_exp_f32 v46, v46\n"
        "v_exp_f32_e64 v47, v47\n"
        "s_add_u32 s75, s78, 0x3000\n"
        "s_and_b32 s75, s75, 0xffff\n"
        "v_exp_f32 v16, v16\n"
        "v_exp_f32 v17, v17\n"
        "v_smfmac_f32_32x32x32_bf16 v[200:215], v[148:151], v[40:47], v10\n"
        "s_mov_b64 exec, s[66:67]\n"
        "buffer_load_dwordx4 v[148:151], v12, s[52:55], s74 offen offset:2560 sc1\n"
        "s_mov_b64 exec, -1\n"
        "v_exp_f32 v18, v18\n"
        "v_exp_f32 v19, v19\n"
        "v_exp_f32 v20, v20\n"
        "v_exp_f32 v21, v21\n"
        "v_mfma_f32_32x32x16_bf16 v[32:47], v[152:155], v[180:183], 0\n"
        "buffer_load_dwordx4 v[152:155], v11, s[52:55], s75 offen offset:0 sc1\n"
        "v_exp_f32 v22, v22\n"
        "v_exp_f32 v23, v23\n"
        "v_exp_f32 v24, v24\n"
        "v_exp_f32 v25, v25\n"
        "v_exp_f32 v26, v26\n"
        "s_waitcnt vmcnt(11)\n"
        "v_smfmac_f32_32x32x32_bf16 v[184:199], v[156:159], v[16:23], v10\n"
        "v_exp_f32 v27, v27\n"
        "v_exp_f32 v28, v28\n"
        "v_exp_f32 v29, v29\n"
        "v_exp_f32 v30, v30\n"
        "v_exp_f32 v31, v31\n"
        "v_exp_f32 v32, v32\n"
        "v_exp_f32 v33, v33\n"
        "s_waitcnt vmcnt(10)\n"
        "v_smfmac_f32_32x32x32_bf16 v[184:199], v[160:163], v[24:31], v10\n"
        "v_exp_f32 v34, v34\n"
        "v_exp_f32 v35, v35\n"
        "v_exp_f32 v36, v36\n"
        "v_exp_f32 v37, v37\n"
        "s_setprio 0\n"
        "s_waitcnt vmcnt(9)\n"
        "v_mfma_f32_32x32x16_bf16 v[16:31], v[164:167], v[176:179], 0\n"
        "v_exp_f32 v38, v38\n"
        "v_exp_f32 v39, v39\n"
        "v_exp_f32 v40, v40\n"
        "v_exp_f32 v41, v41\n"
        "v_exp_f32_e64 v42, v42\n"
        "v_smfmac_f32_32x32x32_bf16 v[200:215], v[156:159], v[32:39], v10\n"
        "s_mov_b64 exec, s[66:67]\n"
        "buffer_load_dwordx4 v[156:159], v12, s[52:55], s75 offen offset:0 sc1\n"
        "s_mov_b64 exec, -1\n"
        "v_exp_f32 v43, v43\n"
        "v_exp_f32 v44, v44\n"
        "v_exp_f32 v45, v45\n"
        "v_exp_f32 v46, v46\n"
        "v_exp_f32 v47, v47\n"
        "v_exp_f32 v16, v16\n"
        "v_exp_f32_e64 v17, v17\n"
        "v_smfmac_f32_32x32x32_bf16 v[200:215], v[160:163], v[40:47], v10\n"
        "s_mov_b64 exec, s[66:67]\n"
        "buffer_load_dwordx4 v[160:163], v12, s[52:55], s75 offen offset:512 sc1\n"
        "s_mov_b64 exec, -1\n"
        "v_exp_f32 v18, v18\n"
        "v_exp_f32 v19, v19\n"
        "s_mov_b32 s68, 0x500000\n"
        "buffer_store_dwordx4 v[48:51], v255, s[52:55], s68 offen sc1\n"
        "v_exp_f32 v20, v20\n"
        "v_exp_f32 v21, v21\n"
        "v_mfma_f32_32x32x16_bf16 v[32:47], v[164:167], v[180:183], 0\n"
        "buffer_load_dwordx4 v[164:167], v11, s[52:55], s75 offen offset:2048 sc1\n"
        "v_exp_f32 v22, v22\n"
        "v_exp_f32 v23, v23\n"
        "v_xor_b32 v253, 128, v254\n"
        "v_exp_f32 v24, v24\n"
        "v_exp_f32 v25, v25\n"
        "v_exp_f32 v26, v26\n"
        "s_waitcnt vmcnt(12)\n"
        "v_smfmac_f32_32x32x32_bf16 v[184:199], v[168:171], v[16:23], v10\n"
        "v_exp_f32 v27, v27\n"
        "v_exp_f32 v28, v28\n"
        "v_exp_f32 v29, v29\n"
        "v_exp_f32 v30, v30\n"
        "v_exp_f32 v31, v31\n"
        "v_exp_f32 v32, v32\n"
        "v_exp_f32 v33, v33\n"
        "s_waitcnt vmcnt(11)\n"
        "v_smfmac_f32_32x32x32_bf16 v[184:199], v[172:175], v[24:31], v10\n"
        "v_exp_f32 v34, v34\n"
        "v_exp_f32 v35, v35\n"
        "v_exp_f32_e64 v36, v36\n"
        "v_exp_f32 v37, v37\n"
        "s_waitcnt vmcnt(10)\n"
        "v_mfma_f32_32x32x16_bf16 v[16:31], v[128:131], v[176:179], 0\n"
        "v_exp_f32 v38, v38\n"
        "v_exp_f32 v39, v39\n"
        "v_exp_f32 v40, v40\n"
        "v_exp_f32 v41, v41\n"
        "v_exp_f32_e64 v42, v42\n"
        "v_smfmac_f32_32x32x32_bf16 v[200:215], v[168:171], v[32:39], v10\n"
        "s_mov_b64 exec, s[66:67]\n"
        "buffer_load_dwordx4 v[168:171], v12, s[52:55], s75 offen offset:2048 sc1\n"
        "s_mov_b64 exec, -1\n"
        "v_exp_f32 v43, v43\n"
        "v_exp_f32 v44, v44\n"
        "v_exp_f32 v45, v45\n"
        "v_exp_f32 v46, v46\n"
        "v_exp_f32_e64 v47, v47\n"
        "s_add_u32 s74, s78, 0x4000\n"
        "s_and_b32 s74, s74, 0xffff\n"
        "s_cbranch_vccz L_p128_%=\n"
        "s_setprio 1\n"
        "L_p128_%=:\n"
        "v_exp_f32 v16, v16\n"
        "v_exp_f32 v17, v17\n"
        "v_smfmac_f32_32x32x32_bf16 v[200:215], v[172:175], v[40:47], v10\n"
        "s_mov_b64 exec, s[66:67]\n"
        "buffer_load_dwordx4 v[172:175], v12, s[52:55], s75 offen offset:2560 sc1\n"
        "s_mov_b64 exec, -1\n"
        "v_exp_f32 v18, v18\n"
        "v_exp_f32 v19, v19\n"
        "v_exp_f32 v20, v20\n"
        "v_exp_f32 v21, v21\n"
        "v_mfma_f32_32x32x16_bf16 v[32:47], v[128:131], v[180:183], 0\n"
        "buffer_load_dwordx4 v[128:131], v11, s[52:55], s74 offen offset:0 sc1\n"
        "v_exp_f32 v22, v22\n"
        "v_exp_f32 v23, v23\n"
        "v_exp_f32 v24, v24\n"
        "v_exp_f32 v25, v25\n"
        "v_exp_f32 v26, v26\n"
        "s_waitcnt vmcnt(12)\n"
        "v_smfmac_f32_32x32x32_bf16 v[184:199], v[132:135], v[16:23], v10\n"
        "v_exp_f32 v27, v27\n"
        "v_exp_f32 v28, v28\n"
        "v_exp_f32 v29, v29\n"
        "v_exp_f32 v30, v30\n"
        "v_exp_f32 v31, v31\n"
        "v_exp_f32 v32, v32\n"
        "v_exp_f32 v33, v33\n"
        "s_waitcnt vmcnt(11)\n"
        "v_smfmac_f32_32x32x32_bf16 v[184:199], v[136:139], v[24:31], v10\n"
        "v_exp_f32 v34, v34\n"
        "v_exp_f32 v35, v35\n"
        "v_exp_f32_e64 v36, v36\n"
        "v_exp_f32 v37, v37\n"
        "s_waitcnt vmcnt(10)\n"
        "v_mfma_f32_32x32x16_bf16 v[16:31], v[140:143], v[176:179], 0\n"
        "v_exp_f32 v38, v38\n"
        "v_exp_f32 v39, v39\n"
        "s_cbranch_vccz L_fp_%=\n"
        "L_fp_b_%=:\n"
        "v_exp_f32 v40, v40\n"
        "v_exp_f32 v41, v41\n"
        "v_exp_f32 v42, v42\n"
        "v_smfmac_f32_32x32x32_bf16 v[200:215], v[132:135], v[32:39], v10\n"
        "s_mov_b64 exec, s[66:67]\n"
        "buffer_load_dwordx4 v[132:135], v12, s[52:55], s74 offen offset:0 sc1\n"
        "s_mov_b64 exec, -1\n"
        "v_exp_f32 v43, v43\n"
        "v_exp_f32 v44, v44\n"
        "v_exp_f32 v45, v45\n"
        "v_exp_f32 v46, v46\n"
        "v_exp_f32 v47, v47\n"
        "v_exp_f32 v16, v16\n"
        "v_exp_f32_e64 v17, v17\n"
        "v_smfmac_f32_32x32x32_bf16 v[200:215], v[136:139], v[40:47], v10\n"
        "s_mov_b64 exec, s[66:67]\n"
        "buffer_load_dwordx4 v[136:139], v12, s[52:55], s74 offen offset:512 sc1\n"
        "s_mov_b64 exec, -1\n"
        "v_exp_f32 v18, v18\n"
        "v_exp_f32 v19, v19\n"
        "v_exp_f32 v20, v20\n"
        "v_exp_f32 v21, v21\n"
        "v_mfma_f32_32x32x16_bf16 v[32:47], v[140:143], v[180:183], 0\n"
        "buffer_load_dwordx4 v[140:143], v11, s[52:55], s74 offen offset:2048 sc1\n"
        "v_exp_f32 v22, v22\n"
        "v_exp_f32 v23, v23\n"
        "v_exp_f32 v24, v24\n"
        "v_exp_f32 v25, v25\n"
        "v_exp_f32 v26, v26\n"
        "s_waitcnt vmcnt(12)\n"
        "v_smfmac_f32_32x32x32_bf16 v[184:199], v[144:147], v[16:23], v10\n"
        "v_exp_f32 v27, v27\n"
        "v_exp_f32 v28, v28\n"
        "v_exp_f32 v29, v29\n"
        "v_exp_f32 v30, v30\n"
        "v_exp_f32 v31, v31\n"
        "v_exp_f32 v32, v32\n"
        "v_exp_f32 v33, v33\n"
        "s_waitcnt vmcnt(11)\n"
        "v_smfmac_f32_32x32x32_bf16 v[184:199], v[148:151], v[24:31], v10\n"
        "v_exp_f32 v34, v34\n"
        "v_exp_f32 v35, v35\n"
        "v_exp_f32_e64 v36, v36\n"
        "v_exp_f32 v37, v37\n"
        "s_waitcnt vmcnt(10)\n"
        "v_mfma_f32_32x32x16_bf16 v[16:31], v[152:155], v[176:179], 0\n"
        "v_exp_f32 v38, v38\n"
        "v_exp_f32 v39, v39\n"
        "v_exp_f32 v40, v40\n"
        "v_exp_f32 v41, v41\n"
        "v_exp_f32_e64 v42, v42\n"
        "v_smfmac_f32_32x32x32_bf16 v[200:215], v[144:147], v[32:39], v10\n"
        "s_mov_b64 exec, s[66:67]\n"
        "buffer_load_dwordx4 v[144:147], v12, s[52:55], s74 offen offset:2048 sc1\n"
        "s_mov_b64 exec, -1\n"
        "v_exp_f32 v43, v43\n"
        "v_exp_f32 v44, v44\n"
        "v_exp_f32 v45, v45\n"
        "v_exp_f32 v46, v46\n"
        "v_exp_f32_e64 v47, v47\n"
        "s_add_u32 s75, s78, 0x5000\n"
        "s_and_b32 s75, s75, 0xffff\n"
        "v_exp_f32 v16, v16\n"
        "v_exp_f32 v17, v17\n"
        "v_smfmac_f32_32x32x32_bf16 v[200:215], v[148:151], v[40:47], v10\n"
        "s_mov_b64 exec, s[66:67]\n"
        "buffer_load_dwordx4 v[148:151], v12, s[52:55], s74 offen offset:2560 sc1\n"
        "s_mov_b64 exec, -1\n"
        "v_exp_f32 v18, v18\n"
        "v_exp_f32 v19, v19\n"
        "v_exp_f32 v20, v20\n"
        "v_exp_f32 v21, v21\n"
        "v_mfma_f32_32x32x16_bf16 v[32:47], v[152:155], v[180:183], 0\n"
        "buffer_load_dwordx4 v[152:155], v11, s[52:55], s75 offen offset:0 sc1\n"
        "v_exp_f32 v22, v22\n"
        "v_exp_f32 v23, v23\n"
        "v_exp_f32 v24, v24\n"
        "v_exp_f32 v25, v25\n"
        "v_exp_f32 v26, v26\n"
        "s_waitcnt vmcnt(12)\n"
        "v_smfmac_f32_32x32x32_bf16 v[184:199], v[156:159], v[16:23], v10\n"
        "v_exp_f32 v27, v27\n"
        "v_exp_f32 v28, v28\n"
        "v_exp_f32 v29, v29\n"
        "v_exp_f32 v30, v30\n"
        "v_exp_f32 v31, v31\n"
        "v_exp_f32 v32, v32\n"
        "v_exp_f32 v33, v33\n"
        "s_waitcnt vmcnt(11)\n"
        "v_smfmac_f32_32x32x32_bf16 v[184:199], v[160:163], v[24:31], v10\n"
        "v_exp_f32 v34, v34\n"
        "v_exp_f32 v35, v35\n"
        "v_exp_f32 v36, v36\n"
        "v_exp_f32 v37, v37\n"
        "s_setprio 0\n"
        "s_waitcnt vmcnt(9)\n"
        "v_mfma_f32_32x32x16_bf16 v[16:31], v[164:167], v[176:179], 0\n"
        "v_exp_f32 v38, v38\n"
        "v_exp_f32 v39, v39\n"
        "v_exp_f32 v40, v40\n"
        "v_exp_f32 v41, v41\n"
        "v_exp_f32_e64 v42, v42\n"
        "v_smfmac_f32_32x32x32_bf16 v[200:215], v[156:159], v[32:39], v10\n"
        "s_mov_b64 exec, s[66:67]\n"
        "buffer_load_dwordx4 v[156:159], v12, s[52:55], s75 offen offset:0 sc1\n"
        "s_mov_b64 exec, -1\n"
        "v_exp_f32 v43, v43\n"
        "v_exp_f32 v44, v44\n"
        "v_exp_f32 v45, v45\n"
        "v_exp_f32 v46, v46\n"
        "v_exp_f32 v47, v47\n"
        "v_exp_f32 v16, v16\n"
        "v_exp_f32_e64 v17, v17\n"
        "v_smfmac_f32_32x32x32_bf16 v[200:215], v[160:163], v[40:47], v10\n"
        "s_mov_b64 exec, s[66:67]\n"
        "buffer_load_dwordx4 v[160:163], v12, s[52:55], s75 offen offset:512 sc1\n"
        "s_mov_b64 exec, -1\n"
        "v_exp_f32 v18, v18\n"
        "v_exp_f32 v19, v19\n"
        "v_exp_f32 v20, v20\n"
        "v_exp_f32 v21, v21\n"
        "v_mfma_f32_32x32x16_bf16 v[32:47], v[164:167], v[180:183], 0\n"
        "buffer_load_dwordx4 v[164:167], v11, s[52:55], s75 offen offset:2048 sc1\n"
        "v_exp_f32 v22, v22\n"
        "v_exp_f32 v23, v23\n"
        "v_exp_f32 v24, v24\n"
        "v_exp_f32 v25, v25\n"
        "v_exp_f32 v26, v26\n"
        "s_waitcnt vmcnt(11)\n"
        "v_smfmac_f32_32x32x32_bf16 v[184:199], v[168:171], v[16:23], v10\n"
        "v_exp_f32 v27, v27\n"
        "v_exp_f32 v28, v28\n"
        "v_exp_f32 v29, v29\n"
        "v_exp_f32 v30, v30\n"
        "v_exp_f32 v31, v31\n"
        "v_exp_f32 v32, v32\n"
        "v_exp_f32 v33, v33\n"
        "s_waitcnt vmcnt(10)\n"
        "v_smfmac_f32_32x32x32_bf16 v[184:199], v[172:175], v[24:31], v10\n"
        "v_exp_f32 v34, v34\n"
        "v_exp_f32 v35, v35\n"
        "v_exp_f32_e64 v36, v36\n"
        "v_exp_f32 v37, v37\n"
        "s_waitcnt vmcnt(9)\n"
        "v_mfma_f32_32x32x16_bf16 v[16:31], v[128:131], v[176:179], 0\n"
        "v_exp_f32 v38, v38\n"
        "v_exp_f32 v39, v39\n"
        "s_cmp_eq_u32 s60, 7\n"
        "s_cbranch_scc1 L_pp_%=\n"
        "L_pp_b_%=:\n"
        "v_exp_f32 v40, v40\n"
        "v_exp_f32 v41, v41\n"
        "v_exp_f32_e64 v42, v42\n"
        "v_smfmac_f32_32x32x32_bf16 v[200:215], v[168:171], v[32:39], v10\n"
        "s_mov_b64 exec, s[66:67]\n"
        "buffer_load_dwordx4 v[168:171], v12, s[52:55], s75 offen offset:2048 sc1\n"
        "s_mov_b64 exec, -1\n"
        "v_exp_f32 v43, v43\n"
        "v_exp_f32 v44, v44\n"
        "v_exp_f32 v45, v45\n"
        "v_exp_f32 v46, v46\n"
        "v_exp_f32_e64 v47, v47\n"
        "s_add_u32 s74, s78, 0x6000\n"
        "s_and_b32 s74, s74, 0xffff\n"
        "s_cbranch_vccz L_p256_%=\n"
        "s_setprio 1\n"
        "L_p256_%=:\n"
        "v_exp_f32 v16, v16\n"
        "v_exp_f32 v17, v17\n"
        "v_smfmac_f32_32x32x32_bf16 v[200:215], v[172:175], v[40:47], v10\n"
        "s_mov_b64 exec, s[66:67]\n"
        "buffer_load_dwordx4 v[172:175], v12, s[52:55], s75 offen offset:2560 sc1\n"
        "s_mov_b64 exec, -1\n"
        "v_exp_f32 v18, v18\n"
        "v_exp_f32 v19, v19\n"
        "v_exp_f32 v20, v20\n"
        "v_exp_f32 v21, v21\n"
        "v_mfma_f32_32x32x16_bf16 v[32:47], v[128:131], v[180:183], 0\n"
        "buffer_load_dwordx4 v[128:131], v11, s[52:55], s74 offen offset:0 sc1\n"
        "v_exp_f32 v22, v22\n"
        "v_exp_f32 v23, v23\n"
        "v_exp_f32 v24, v24\n"
        "v_exp_f32 v25, v25\n"
        "v_exp_f32 v26, v26\n"
        "s_waitcnt vmcnt(11)\n"
        "v_smfmac_f32_32x32x32_bf16 v[184:199], v[132:135], v[16:23], v10\n"
        "v_exp_f32 v27, v27\n"
        "v_exp_f32 v28, v28\n"
        "v_exp_f32 v29, v29\n"
        "v_exp_f32 v30, v30\n"
        "v_exp_f32 v31, v31\n"
        "v_exp_f32 v32, v32\n"
        "v_exp_f32 v33, v33\n"
        "s_waitcnt vmcnt(10)\n"
        "v_smfmac_f32_32x32x32_bf16 v[184:199], v[136:139], v[24:31], v10\n"
        "v_exp_f32 v34, v34\n"
        "v_exp_f32 v35, v35\n"
        "v_exp_f32_e64 v36, v36\n"
        "v_exp_f32 v37, v37\n"
        "s_waitcnt vmcnt(9)\n"
        "v_mfma_f32_32x32x16_bf16 v[16:31], v[140:143], v[176:179], 0\n"
        "v_exp_f32 v38, v38\n"
        "v_exp_f32 v39, v39\n"
        "v_exp_f32 v40, v40\n"
        "v_exp_f32 v41, v41\n"
        "v_exp_f32_e64 v42, v42\n"
        "v_smfmac_f32_32x32x32_bf16 v[200:215], v[132:135], v[32:39], v10\n"
        "s_mov_b64 exec, s[66:67]\n"
        "buffer_load_dwordx4 v[132:135], v12, s[52:55], s74 offen offset:0 sc1\n"
        "s_mov_b64 exec, -1\n"
        "v_exp_f32 v43, v43\n"
        "v_exp_f32 v44, v44\n"
        "v_exp_f32 v45, v45\n"
        "v_exp_f32 v46, v46\n"
        "v_exp_f32 v47, v47\n"
        "v_exp_f32 v16, v16\n"
        "v_exp_f32_e64 v17, v17\n"
        "v_smfmac_f32_32x32x32_bf16 v[200:215], v[136:139], v[40:47], v10\n"
        "s_mov_b64 exec, s[66:67]\n"
        "buffer_load_dwordx4 v[136:139], v12, s[52:55], s74 offen offset:512 sc1\n"
        "s_mov_b64 exec, -1\n"
        "v_exp_f32_e64 v18, v18\n"
        "v_exp_f32 v19, v19\n"
        "s_cmp_eq_u32 s60, 7\n"
        "s_cbranch_scc1 L_pc_%=\n"
        "L_pc_d_%=:\n"
        "s_barrier\n"
        "ds_read_b128 v[216:219], v226 offset:8192\n"
        "v_exp_f32 v20, v20\n"
        "v_exp_f32 v21, v21\n"
        "v_mfma_f32_32x32x16_bf16 v[32:47], v[140:143], v[180:183], 0\n"
        "buffer_load_dwordx4 v[140:143], v11, s[52:55], s74 offen offset:2048 sc1\n"
        "v_exp_f32 v22, v22\n"
        "v_exp_f32 v23, v23\n"
        "s_cmp_lt_u32 s60, 2\n"
        "s_cbranch_scc1 L_red1_%=\n"
        "L_red1_b_%=:\n"
        "v_exp_f32 v24, v24\n"
        "v_exp_f32 v25, v25\n"
        "v_exp_f32 v26, v26\n"
        "s_waitcnt vmcnt(11)\n"
        "v_smfmac_f32_32x32x32_bf16 v[184:199], v[144:147], v[16:23], v10\n"
        "v_exp_f32 v27, v27\n"
        "v_exp_f32 v28, v28\n"
        "v_exp_f32 v29, v29\n"
        "v_exp_f32 v30, v30\n"
        "v_exp_f32 v31, v31\n"
        "v_exp_f32 v32, v32\n"
        "v_exp_f32 v33, v33\n"
        "s_waitcnt vmcnt(10)\n"
        "v_smfmac_f32_32x32x32_bf16 v[184:199], v[148:151], v[24:31], v10\n"
        "v_exp_f32 v34, v34\n"
        "v_exp_f32 v35, v35\n"
        "v_exp_f32_e64 v36, v36\n"
        "v_exp_f32 v37, v37\n"
        "s_waitcnt vmcnt(9)\n"
        "v_mfma_f32_32x32x16_bf16 v[16:31], v[152:155], v[176:179], 0\n"
        "v_exp_f32 v38, v38\n"
        "v_exp_f32 v39, v39\n"
        "v_exp_f32 v40, v40\n"
        "s_add_u32 s76, s76, 1\n"
        "s_and_b32 s76, s76, 7\n"
        "s_lshl_b32 s79, s76, 13\n"
        "s_add_u32 m0, s79, s33\n"
        "s_lshr_b32 s68, s76, 1\n"
        "s_lshl_b32 s68, s68, 17\n"
        "s_bitcmp1_b32 s76, 0\n"
        "s_cselect_b32 s69, s13, s12\n"
        "s_add_u32 s79, s68, s69\n"
        "buffer_load_dwordx4 v254, s[44:47], s79 offen lds\n"
        "v_exp_f32 v41, v41\n"
        "v_exp_f32 v42, v42\n"
        "v_smfmac_f32_32x32x32_bf16 v[200:215], v[144:147], v[32:39], v10\n"
        "s_mov_b64 exec, s[66:67]\n"
        "buffer_load_dwordx4 v[144:147], v12, s[52:55], s74 offen offset:2048 sc1\n"
        "s_mov_b64 exec, -1\n"
        "v_exp_f32 v43, v43\n"
        "v_exp_f32 v44, v44\n"
        "s_add_u32 m0, m0, 0x10000\n"
        "s_nop 0\n"
        "buffer_load_dwordx4 v253, s[40:43], s79 offen lds\n"
        "v_exp_f32 v45, v45\n"
        "v_exp_f32 v46, v46\n"
        "v_exp_f32 v47, v47\n"
        "s_add_u32 s75, s78, 0x7000\n"
        "s_and_b32 s75, s75, 0xffff\n"
        "v_exp_f32 v16, v16\n"
        "v_exp_f32 v17, v17\n"
        "v_smfmac_f32_32x32x32_bf16 v[200:215], v[148:151], v[40:47], v10\n"
        "s_mov_b64 exec, s[66:67]\n"
        "buffer_load_dwordx4 v[148:151], v12, s[52:55], s74 offen offset:2560 sc1\n"
        "s_mov_b64 exec, -1\n"
        "v_exp_f32 v18, v18\n"
        "v_exp_f32 v19, v19\n"
        "v_exp_f32 v20, v20\n"
        "v_exp_f32 v21, v21\n"
        "v_mfma_f32_32x32x16_bf16 v[32:47], v[152:155], v[180:183], 0\n"
        "buffer_load_dwordx4 v[152:155], v11, s[52:55], s75 offen offset:0 sc1\n"
        "v_exp_f32 v22, v22\n"
        "v_exp_f32 v23, v23\n"
        "v_exp_f32 v24, v24\n"
        "v_exp_f32 v25, v25\n"
        "v_exp_f32 v26, v26\n"
        "s_waitcnt vmcnt(13)\n"
        "v_smfmac_f32_32x32x32_bf16 v[184:199], v[156:159], v[16:23], v10\n"
        "v_exp_f32 v27, v27\n"
        "v_exp_f32 v28, v28\n"
        "v_exp_f32 v29, v29\n"
        "v_exp_f32 v30, v30\n"
        "v_exp_f32 v31, v31\n"
        "v_exp_f32 v32, v32\n"
        "v_exp_f32 v33, v33\n"
        "s_waitcnt vmcnt(12)\n"
        "v_smfmac_f32_32x32x32_bf16 v[184:199], v[160:163], v[24:31], v10\n"
        "v_exp_f32 v34, v34\n"
        "v_exp_f32 v35, v35\n"
        "v_exp_f32 v36, v36\n"
        "v_exp_f32 v37, v37\n"
        "s_setprio 0\n"
        "s_waitcnt vmcnt(11)\n"
        "v_mfma_f32_32x32x16_bf16 v[16:31], v[164:167], v[176:179], 0\n"
        "v_exp_f32 v38, v38\n"
        "v_exp_f32 v39, v39\n"
        "v_exp_f32 v40, v40\n"
        "v_exp_f32 v41, v41\n"
        "v_exp_f32_e64 v42, v42\n"
        "v_smfmac_f32_32x32x32_bf16 v[200:215], v[156:159], v[32:39], v10\n"
        "s_mov_b64 exec, s[66:67]\n"
        "buffer_load_dwordx4 v[156:159], v12, s[52:55], s75 offen offset:0 sc1\n"
        "s_mov_b64 exec, -1\n"
        "v_exp_f32 v43, v43\n"
        "v_exp_f32 v44, v44\n"
        "v_exp_f32 v45, v45\n"
        "v_exp_f32 v46, v46\n"
        "v_exp_f32 v47, v47\n"
        "v_exp_f32 v16, v16\n"
        "v_exp_f32_e64 v17, v17\n"
        "v_smfmac_f32_32x32x32_bf16 v[200:215], v[160:163], v[40:47], v10\n"
        "s_mov_b64 exec, s[66:67]\n"
        "buffer_load_dwordx4 v[160:163], v12, s[52:55], s75 offen offset:512 sc1\n"
        "s_mov_b64 exec, -1\n"
        "v_exp_f32 v18, v18\n"
        "v_exp_f32 v19, v19\n"
        "v_exp_f32 v20, v20\n"
        "v_exp_f32 v21, v21\n"
        "v_mfma_f32_32x32x16_bf16 v[32:47], v[164:167], v[180:183], 0\n"
        "buffer_load_dwordx4 v[164:167], v11, s[52:55], s75 offen offset:2048 sc1\n"
        "v_exp_f32 v22, v22\n"
        "v_exp_f32 v23, v23\n"
        "v_exp_f32 v24, v24\n"
        "v_exp_f32 v25, v25\n"
        "v_exp_f32 v26, v26\n"
        "s_waitcnt vmcnt(13)\n"
        "v_smfmac_f32_32x32x32_bf16 v[184:199], v[168:171], v[16:23], v10\n"
        "v_exp_f32 v27, v27\n"
        "v_exp_f32 v28, v28\n"
        "v_exp_f32 v29, v29\n"
        "v_exp_f32 v30, v30\n"
        "v_exp_f32 v31, v31\n"
        "v_exp_f32 v32, v32\n"
        "v_exp_f32 v33, v33\n"
        "s_waitcnt vmcnt(12)\n"
        "v_smfmac_f32_32x32x32_bf16 v[184:199], v[172:175], v[24:31], v10\n"
        "v_exp_f32 v34, v34\n"
        "v_exp_f32 v35, v35\n"
        "v_exp_f32_e64 v36, v36\n"
        "v_exp_f32 v37, v37\n"
        "s_waitcnt vmcnt(11)\n"
        "v_mfma_f32_32x32x16_bf16 v[16:31], v[128:131], v[176:179], 0\n"
        "v_exp_f32 v38, v38\n"
        "v_exp_f32 v39, v39\n"
        "v_exp_f32 v40, v40\n"
        "v_exp_f32 v41, v41\n"
        "v_exp_f32_e64 v42, v42\n"
        "v_smfmac_f32_32x32x32_bf16 v[200:215], v[168:171], v[32:39], v10\n"
        "s_mov_b64 exec, s[66:67]\n"
        "buffer_load_dwordx4 v[168:171], v12, s[52:55], s75 offen offset:2048 sc1\n"
        "s_mov_b64 exec, -1\n"
        "v_exp_f32 v43, v43\n"
        "v_exp_f32 v44, v44\n"
        "v_exp_f32 v45, v45\n"
        "s_cmp_lt_u32 s60, 2\n"
        "s_cbranch_scc1 L_red2_%=\n"
        "L_red2_b_%=:\n"
        "v_exp_f32 v46, v46\n"
        "v_exp_f32_e64 v47, v47\n"
        "s_add_u32 s74, s78, 0x8000\n"
        "s_and_b32 s74, s74, 0xffff\n"
        "s_cbranch_vccz L_p384_%=\n"
        "s_setprio 1\n"
        "L_p384_%=:\n"
        "v_exp_f32 v16, v16\n"
        "v_exp_f32 v17, v17\n"
        "v_smfmac_f32_32x32x32_bf16 v[200:215], v[172:175], v[40:47], v10\n"
        "s_mov_b64 exec, s[66:67]\n"
        "buffer_load_dwordx4 v[172:175], v12, s[52:55], s75 offen offset:2560 sc1\n"
        "s_mov_b64 exec, -1\n"
        "v_exp_f32 v18, v18\n"
        "v_exp_f32 v19, v19\n"
        "v_exp_f32 v20, v20\n"
        "v_exp_f32 v21, v21\n"
        "v_mfma_f32_32x32x16_bf16 v[32:47], v[128:131], v[180:183], 0\n"
        "buffer_load_dwordx4 v[128:131], v11, s[52:55], s74 offen offset:0 sc1\n"
        "v_exp_f32 v22, v22\n"
        "v_exp_f32 v23, v23\n"
        "v_exp_f32 v24, v24\n"
        "s_add_u32 s76, s76, 1\n"
        "s_and_b32 s76, s76, 7\n"
        "s_lshl_b32 s79, s76, 13\n"
        "s_add_u32 m0, s79, s33\n"
        "s_lshr_b32 s68, s76, 1\n"
        "s_lshl_b32 s68, s68, 17\n"
        "s_bitcmp1_b32 s76, 0\n"
        "s_cselect_b32 s69, s13, s12\n"
        "s_add_u32 s79, s68, s69\n"
        "buffer_load_dwordx4 v254, s[44:47], s79 offen lds\n"
        "v_exp_f32_e64 v25, v25\n"
        "v_exp_f32 v26, v26\n"
        "s_waitcnt vmcnt(14)\n"
        "v_smfmac_f32_32x32x32_bf16 v[184:199], v[132:135], v[16:23], v10\n"
        "v_exp_f32 v27, v27\n"
        "v_exp_f32 v28, v28\n"
        "s_add_u32 m0, m0, 0x10000\n"
        "s_nop 0\n"
        "buffer_load_dwordx4 v253, s[40:43], s79 offen lds\n"
        "v_exp_f32 v29, v29\n"
        "v_exp_f32 v30, v30\n"
        "v_exp_f32 v31, v31\n"
        "v_exp_f32_e64 v32, v32\n"
        "v_exp_f32 v33, v33\n"
        "s_waitcnt vmcnt(14)\n"
        "v_smfmac_f32_32x32x32_bf16 v[184:199], v[136:139], v[24:31], v10\n"
        "v_exp_f32 v34, v34\n"
        "v_exp_f32 v35, v35\n"
        "v_exp_f32_e64 v36, v36\n"
        "v_exp_f32 v37, v37\n"
        "s_waitcnt vmcnt(13)\n"
        "v_mfma_f32_32x32x16_bf16 v[16:31], v[140:143], v[176:179], 0\n"
        "v_exp_f32 v38, v38\n"
        "v_exp_f32 v39, v39\n"
        "v_exp_f32 v40, v40\n"
        "v_exp_f32 v41, v41\n"
        "v_exp_f32_e64 v42, v42\n"
        "v_smfmac_f32_32x32x32_bf16 v[200:215], v[132:135], v[32:39], v10\n"
        "s_mov_b64 exec, s[66:67]\n"
        "buffer_load_dwordx4 v[132:135], v12, s[52:55], s74 offen offset:0 sc1\n"
        "s_mov_b64 exec, -1\n"
        "v_exp_f32 v43, v43\n"
        "v_exp_f32 v44, v44\n"
        "v_exp_f32 v45, v45\n"
        "v_exp_f32 v46, v46\n"
        "v_exp_f32 v47, v47\n"
        "v_exp_f32 v16, v16\n"
        "v_exp_f32_e64 v17, v17\n"
        "v_smfmac_f32_32x32x32_bf16 v[200:215], v[136:139], v[40:47], v10\n"
        "s_mov_b64 exec, s[66:67]\n"
        "buffer_load_dwordx4 v[136:139], v12, s[52:55], s74 offen offset:512 sc1\n"
        "s_mov_b64 exec, -1\n"
        "v_exp_f32 v18, v18\n"
        "v_exp_f32 v19, v19\n"
        "v_exp_f32 v20, v20\n"
        "v_exp_f32 v21, v21\n"
        "v_mfma_f32_32x32x16_bf16 v[32:47], v[140:143], v[180:183], 0\n"
        "buffer_load_dwordx4 v[140:143], v11, s[52:55], s74 offen offset:2048 sc1\n"
        "v_exp_f32 v22, v22\n"
        "v_exp_f32 v23, v23\n"
        "v_exp_f32 v24, v24\n"
        "s_add_u32 s76, s76, 1\n"
        "s_and_b32 s76, s76, 7\n"
        "s_lshl_b32 s79, s76, 13\n"
        "s_add_u32 m0, s79, s33\n"
        "s_lshr_b32 s68, s76, 1\n"
        "s_lshl_b32 s68, s68, 17\n"
        "s_bitcmp1_b32 s76, 0\n"
        "s_cselect_b32 s69, s13, s12\n"
        "s_add_u32 s79, s68, s69\n"
        "buffer_load_dwordx4 v254, s[44:47], s79 offen lds\n"
        "v_exp_f32_e64 v25, v25\n"
        "v_exp_f32 v26, v26\n"
        "s_waitcnt vmcnt(15)\n"
        "v_smfmac_f32_32x32x32_bf16 v[184:199], v[144:147], v[16:23], v10\n"
        "v_exp_f32 v27, v27\n"
        "v_exp_f32 v28, v28\n"
        "s_add_u32 m0, m0, 0x10000\n"
        "s_nop 0\n"
        "buffer_load_dwordx4 v253, s[40:43], s79 offen lds\n"
        "v_exp_f32 v29, v29\n"
        "v_exp_f32 v30, v30\n"
        "v_exp_f32 v31, v31\n"
        "v_exp_f32_e64 v32, v32\n"
        "v_exp_f32 v33, v33\n"
        "s_waitcnt vmcnt(14)\n"
        "v_smfmac_f32_32x32x32_bf16 v[184:199], v[148:151], v[24:31], v10\n"
        "v_exp_f32 v34, v34\n"
        "v_exp_f32 v35, v35\n"
        "v_exp_f32_e64 v36, v36\n"
        "v_exp_f32 v37, v37\n"
        "s_waitcnt vmcnt(13)\n"
        "v_mfma_f32_32x32x16_bf16 v[16:31], v[152:155], v[176:179], 0\n"
        "v_exp_f32 v38, v38\n"
        "v_exp_f32 v39, v39\n"
        "v_exp_f32 v40, v40\n"
        "v_exp_f32 v41, v41\n"
        "v_exp_f32_e64 v42, v42\n"
        "v_smfmac_f32_32x32x32_bf16 v[200:215], v[144:147], v[32:39], v10\n"
        "s_mov_b64 exec, s[66:67]\n"
        "buffer_load_dwordx4 v[144:147], v12, s[52:55], s74 offen offset:2048 sc1\n"
        "s_mov_b64 exec, -1\n"
        "v_exp_f32 v43, v43\n"
        "v_exp_f32 v44, v44\n"
        "v_exp_f32 v45, v45\n"
        "v_exp_f32 v46, v46\n"
        "v_exp_f32_e64 v47, v47\n"
        "s_add_u32 s75, s78, 0x9000\n"
        "s_and_b32 s75, s75, 0xffff\n"
        "v_exp_f32 v16, v16\n"
        "v_exp_f32 v17, v17\n"
        "v_smfmac_f32_32x32x32_bf16 v[200:215], v[148:151], v[40:47], v10\n"
        "s_mov_b64 exec, s[66:67]\n"
        "buffer_load_dwordx4 v[148:151], v12, s[52:55], s74 offen offset:2560 sc1\n"
        "s_mov_b64 exec, -1\n"
        "v_exp_f32 v18, v18\n"
        "v_exp_f32 v19, v19\n"
        "v_exp_f32 v20, v20\n"
        "v_exp_f32 v21, v21\n"
        "v_mfma_f32_32x32x16_bf16 v[32:47], v[152:155], v[180:183], 0\n"
        "buffer_load_dwordx4 v[152:155], v11, s[52:55], s75 offen offset:0 sc1\n"
        "v_exp_f32 v22, v22\n"
        "v_exp_f32 v23, v23\n"
        "v_exp_f32 v24, v24\n"
        "s_add_u32 s76, s76, 1\n"
        "s_and_b32 s76, s76, 7\n"
        "s_lshl_b32 s79, s76, 13\n"
        "s_add_u32 m0, s79, s33\n"
        "s_lshr_b32 s68, s76, 1\n"
        "s_lshl_b32 s68, s68, 17\n"
        "s_bitcmp1_b32 s76, 0\n"
        "s_cselect_b32 s69, s13, s12\n"
        "s_add_u32 s79, s68, s69\n"
        "buffer_load_dwordx4 v254, s[44:47], s79 offen lds\n"
        "v_exp_f32_e64 v25, v25\n"
        "v_exp_f32 v26, v26\n"
        "s_waitcnt vmcnt(16)\n"
        "v_smfmac_f32_32x32x32_bf16 v[184:199], v[156:159], v[16:23], v10\n"
        "v_exp_f32 v27, v27\n"
        "v_exp_f32 v28, v28\n"
        "s_add_u32 m0, m0, 0x10000\n"
        "s_nop 0\n"
        "buffer_load_dwordx4 v253, s[40:43], s79 offen lds\n"
        "v_exp_f32 v29, v29\n"
        "v_exp_f32 v30, v30\n"
        "v_exp_f32 v31, v31\n"
        "s_mul_i32 s68, s62, 4352\n"
        "s_add_u32 s68, s68, 0xb00000\n"
        "s_and_b32 s69, s60, 3\n"
        "s_lshl_b32 s70, s69, 6\n"
        "s_add_u32 s70, s70, s68\n"
        "s_add_u32 s70, s70, 0x1000\n"
        "s_lshl_b32 s69, s69, 10\n"
        "s_add_u32 s71, s68, s69\n"
        "v_exp_f32 v32, v32\n"
        "v_lshrrev_b32_e64 v113, 5, v1\n"
        "v_exp_f32 v33, v33\n"
        "s_waitcnt vmcnt(16)\n"
        "v_smfmac_f32_32x32x32_bf16 v[184:199], v[160:163], v[24:31], v10\n"
        "v_and_b32 v114, 15, v1\n"
        "v_exp_f32 v34, v34\n"
        "v_lshl_add_u32 v114, v113, 4, v114\n"
        "v_exp_f32_e64 v35, v35\n"
        "v_lshl_add_u32 v114, v114, 5, s71\n"
        "v_exp_f32_e64 v36, v36\n"
        "v_lshl_add_u32 v113, v113, 5, s70\n"
        "v_exp_f32_e64 v37, v37\n"
        "s_setprio 0\n"
        "s_waitcnt vmcnt(15)\n"
        "v_mfma_f32_32x32x16_bf16 v[16:31], v[164:167], v[176:179], 0\n"
        "v_cndmask_b32 v112, v113, v114, s[66:67]\n"
        "v_exp_f32 v38, v38\n"
        "v_exp_f32 v39, v39\n"
        "v_exp_f32 v40, v40\n"
        "v_exp_f32 v41, v41\n"
        "v_exp_f32_e64 v42, v42\n"
        "v_smfmac_f32_32x32x32_bf16 v[200:215], v[156:159], v[32:39], v10\n"
        "s_mov_b64 exec, s[66:67]\n"
        "buffer_load_dwordx4 v[156:159], v12, s[52:55], s75 offen offset:0 sc1\n"
        "s_mov_b64 exec, -1\n"
        "v_exp_f32 v43, v43\n"
        "v_exp_f32 v44, v44\n"
        "v_exp_f32 v45, v45\n"
        "v_exp_f32 v46, v46\n"
        "v_exp_f32 v47, v47\n"
        "v_exp_f32 v16, v16\n"
        "v_exp_f32_e64 v17, v17\n"
        "v_smfmac_f32_32x32x32_bf16 v[200:215], v[160:163], v[40:47], v10\n"
        "s_mov_b64 exec, s[66:67]\n"
        "buffer_load_dwordx4 v[160:163], v12, s[52:55], s75 offen offset:512 sc1\n"
        "s_mov_b64 exec, -1\n"
        "v_exp_f32 v18, v18\n"
        "v_exp_f32 v19, v19\n"
        "v_exp_f32 v20, v20\n"
        "v_exp_f32 v21, v21\n"
        "v_mfma_f32_32x32x16_bf16 v[32:47], v[164:167], v[180:183], 0\n"
        "buffer_load_dwordx4 v[164:167], v11, s[52:55], s75 offen offset:2048 sc1\n"
        "v_exp_f32 v22, v22\n"
        "v_exp_f32 v23, v23\n"
        "v_exp_f32 v24, v24\n"
        "s_add_u32 s76, s76, 1\n"
        "s_and_b32 s76, s76, 7\n"
        "s_lshl_b32 s79, s76, 13\n"
        "s_add_u32 m0, s79, s33\n"
        "s_lshr_b32 s68, s76, 1\n"
        "s_lshl_b32 s68, s68, 17\n"
        "s_bitcmp1_b32 s76, 0\n"
        "s_cselect_b32 s69, s13, s12\n"
        "s_add_u32 s79, s68, s69\n"
        "buffer_load_dwordx4 v254, s[44:47], s79 offen lds\n"
        "v_exp_f32_e64 v25, v25\n"
        "v_exp_f32 v26, v26\n"
        "s_waitcnt vmcnt(18)\n"
        "v_smfmac_f32_32x32x32_bf16 v[184:199], v[168:171], v[16:23], v10\n"
        "v_exp_f32 v27, v27\n"
        "v_exp_f32 v28, v28\n"
        "s_add_u32 m0, m0, 0x10000\n"
        "s_nop 0\n"
        "buffer_load_dwordx4 v253, s[40:43], s79 offen lds\n"
        "v_exp_f32 v29, v29\n"
        "v_exp_f32 v30, v30\n"
        "v_exp_f32 v31, v31\n"
        "v_exp_f32_e64 v32, v32\n"
        "v_exp_f32 v33, v33\n"
        "s_waitcnt vmcnt(18)\n"
        "v_smfmac_f32_32x32x32_bf16 v[184:199], v[172:175], v[24:31], v10\n"
        "v_exp_f32 v34, v34\n"
        "v_exp_f32 v35, v35\n"
        "buffer_load_dwordx4 v[96:99], v112, s[52:55], 0 offen sc1\n"
        "v_exp_f32_e64 v36, v36\n"
        "v_exp_f32 v37, v37\n"
        "s_waitcnt vmcnt(18)\n"
        "v_mfma_f32_32x32x16_bf16 v[16:31], v[128:131], v[176:179], 0\n"
        "v_exp_f32 v38, v38\n"
        "v_exp_f32 v39, v39\n"
        "buffer_load_dwordx4 v[100:103], v112, s[52:55], 0 offen offset:16 sc1\n"
        "v_exp_f32 v40, v40\n"
        "v_exp_f32 v41, v41\n"
        "v_exp_f32_e64 v42, v42\n"
        "v_smfmac_f32_32x32x32_bf16 v[200:215], v[168:171], v[32:39], v10\n"
        "s_mov_b64 exec, s[66:67]\n"
        "buffer_load_dwordx4 v[168:171], v12, s[52:55], s75 offen offset:2048 sc1\n"
        "s_mov_b64 exec, -1\n"
        "v_exp_f32 v43, v43\n"
        "v_exp_f32 v44, v44\n"
        "v_exp_f32 v45, v45\n"
        "v_exp_f32 v46, v46\n"
        "v_exp_f32_e64 v47, v47\n"
        "s_add_u32 s74, s78, 0xa000\n"
        "s_and_b32 s74, s74, 0xffff\n"
        "s_cbranch_vccz L_p512_%=\n"
        "s_setprio 1\n"
        "L_p512_%=:\n"
        "v_mov_b32 v14, v13\n"
        "v_exp_f32 v16, v16\n"
        "v_exp_f32_e64 v17, v17\n"
        "v_smfmac_f32_32x32x32_bf16 v[200:215], v[172:175], v[40:47], v10\n"
        "s_mov_b64 exec, s[66:67]\n"
        "buffer_load_dwordx4 v[172:175], v12, s[52:55], s75 offen offset:2560 sc1\n"
        "s_mov_b64 exec, -1\n"
        "v_exp_f32 v18, v18\n"
        "v_exp_f32 v19, v19\n"
        "v_exp_f32 v20, v20\n"
        "v_exp_f32 v21, v21\n"
        "v_mfma_f32_32x32x16_bf16 v[32:47], v[128:131], v[180:183], 0\n"
        "buffer_load_dwordx4 v[128:131], v11, s[52:55], s74 offen offset:0 sc1\n"
        "v_exp_f32 v22, v22\n"
        "v_exp_f32 v23, v23\n"
        "v_exp_f32 v24, v24\n"
        "s_add_u32 s76, s76, 1\n"
        "s_and_b32 s76, s76, 7\n"
        "s_lshl_b32 s79, s76, 13\n"
        "s_add_u32 m0, s79, s33\n"
        "s_lshr_b32 s68, s76, 1\n"
        "s_lshl_b32 s68, s68, 17\n"
        "s_bitcmp1_b32 s76, 0\n"
        "s_cselect_b32 s69, s13, s12\n"
        "s_add_u32 s79, s68, s69\n"
        "buffer_load_dwordx4 v254, s[44:47], s79 offen lds\n"
        "v_exp_f32_e64 v25, v25\n"
        "v_exp_f32 v26, v26\n"
        "s_waitcnt vmcnt(20)\n"
        "v_smfmac_f32_32x32x32_bf16 v[184:199], v[132:135], v[16:23], v10\n"
        "v_exp_f32 v27, v27\n"
        "v_exp_f32 v28, v28\n"
        "s_add_u32 m0, m0, 0x10000\n"
        "s_nop 0\n"
        "buffer_load_dwordx4 v253, s[40:43], s79 offen lds\n"
        "v_exp_f32 v29, v29\n"
        "v_exp_f32 v30, v30\n"
        "v_exp_f32 v31, v31\n"
        "v_exp_f32_e64 v32, v32\n"
        "v_exp_f32 v33, v33\n"
        "s_waitcnt vmcnt(20)\n"
        "v_smfmac_f32_32x32x32_bf16 v[184:199], v[136:139], v[24:31], v10\n"
        "v_exp_f32 v34, v34\n"
        "v_exp_f32 v35, v35\n"
        "v_exp_f32_e64 v36, v36\n"
        "v_exp_f32 v37, v37\n"
        "s_waitcnt vmcnt(19)\n"
        "v_mfma_f32_32x32x16_bf16 v[16:31], v[140:143], v[176:179], 0\n"
        "v_exp_f32 v38, v38\n"
        "v_exp_f32 v39, v39\n"
        "v_exp_f32 v40, v40\n"
        "v_exp_f32 v41, v41\n"
        "v_exp_f32_e64 v42, v42\n"
        "v_smfmac_f32_32x32x32_bf16 v[200:215], v[132:135], v[32:39], v10\n"
        "s_mov_b64 exec, s[66:67]\n"
        "buffer_load_dwordx4 v[132:135], v12, s[52:55], s74 offen offset:0 sc1\n"
        "s_mov_b64 exec, -1\n"
        "v_exp_f32 v43, v43\n"
        "v_exp_f32 v44, v44\n"
        "v_exp_f32 v45, v45\n"
        "v_exp_f32 v46, v46\n"
        "v_exp_f32 v47, v47\n"
        "v_exp_f32 v16, v16\n"
        "v_exp_f32_e64 v17, v17\n"
        "v_smfmac_f32_32x32x32_bf16 v[200:215], v[136:139], v[40:47], v10\n"
        "s_mov_b64 exec, s[66:67]\n"
        "buffer_load_dwordx4 v[136:139], v12, s[52:55], s74 offen offset:512 sc1\n"
        "s_mov_b64 exec, -1\n"
        "v_exp_f32 v18, v18\n"
        "v_exp_f32 v19, v19\n"
        "v_exp_f32 v20, v20\n"
        "v_exp_f32 v21, v21\n"
        "v_mfma_f32_32x32x16_bf16 v[32:47], v[140:143], v[180:183], 0\n"
        "buffer_load_dwordx4 v[140:143], v11, s[52:55], s74 offen offset:2048 sc1\n"
        "v_exp_f32 v22, v22\n"
        "v_exp_f32 v23, v23\n"
        "v_exp_f32 v24, v24\n"
        "s_add_u32 s76, s76, 1\n"
        "s_and_b32 s76, s76, 7\n"
        "s_lshl_b32 s79, s76, 13\n"
        "s_add_u32 m0, s79, s33\n"
        "s_lshr_b32 s68, s76, 1\n"
        "s_lshl_b32 s68, s68, 17\n"
        "s_bitcmp1_b32 s76, 0\n"
        "s_cselect_b32 s69, s13, s12\n"
        "s_add_u32 s79, s68, s69\n"
        "buffer_load_dwordx4 v254, s[44:47], s79 offen lds\n"
        "v_exp_f32_e64 v25, v25\n"
        "v_exp_f32 v26, v26\n"
        "s_waitcnt vmcnt(20)\n"
        "v_smfmac_f32_32x32x32_bf16 v[184:199], v[144:147], v[16:23], v10\n"
        "v_exp_f32 v27, v27\n"
        "v_exp_f32 v28, v28\n"
        "s_add_u32 m0, m0, 0x10000\n"
        "s_nop 0\n"
        "buffer_load_dwordx4 v253, s[40:43], s79 offen lds\n"
        "v_exp_f32 v29, v29\n"
        "v_exp_f32 v30, v30\n"
        "v_exp_f32 v31, v31\n"
        "v_exp_f32_e64 v32, v32\n"
        "v_exp_f32 v33, v33\n"
        "s_waitcnt vmcnt(20)\n"
        "v_smfmac_f32_32x32x32_bf16 v[184:199], v[148:151], v[24:31], v10\n"
        "v_exp_f32 v34, v34\n"
        "v_exp_f32 v35, v35\n"
        "v_exp_f32_e64 v36, v36\n"
        "v_exp_f32 v37, v37\n"
        "s_waitcnt vmcnt(19)\n"
        "v_mfma_f32_32x32x16_bf16 v[16:31], v[152:155], v[176:179], 0\n"
        "v_exp_f32 v38, v38\n"
        "v_exp_f32 v39, v39\n"
        "v_exp_f32 v40, v40\n"
        "v_exp_f32 v41, v41\n"
        "v_exp_f32_e64 v42, v42\n"
        "v_smfmac_f32_32x32x32_bf16 v[200:215], v[144:147], v[32:39], v10\n"
        "s_mov_b64 exec, s[66:67]\n"
        "buffer_load_dwordx4 v[144:147], v12, s[52:55], s74 offen offset:2048 sc1\n"
        "s_mov_b64 exec, -1\n"
        "v_exp_f32 v43, v43\n"
        "v_exp_f32 v44, v44\n"
        "v_exp_f32 v45, v45\n"
        "v_exp_f32 v46, v46\n"
        "v_exp_f32_e64 v47, v47\n"
        "s_add_u32 s75, s78, 0xb000\n"
        "s_and_b32 s75, s75, 0xffff\n"
        "s_waitcnt vmcnt(11)\n"
        "v_max3_u32 v113, v96, v97, v98\n"
        "v_exp_f32 v16, v16\n"
        "v_max3_u32 v113, v113, v99, v100\n"
        "v_exp_f32_e64 v17, v17\n"
        "v_smfmac_f32_32x32x32_bf16 v[200:215], v[148:151], v[40:47], v10\n"
        "s_mov_b64 exec, s[66:67]\n"
        "buffer_load_dwordx4 v[148:151], v12, s[52:55], s74 offen offset:2560 sc1\n"
        "s_mov_b64 exec, -1\n"
        "v_max3_u32 v113, v113, v101, v102\n"
        "v_exp_f32 v18, v18\n"
        "v_max_u32 v113, v113, v103\n"
        "v_exp_f32 v19, v19\n"
        "v_exp_f32 v20, v20\n"
        "v_cmp_eq_u32 s[68:69], -1, v113\n"
        "s_nop 0\n"
        "s_cmp_lg_u64 s[68:69], 0\n"
        "s_cbranch_scc1 L_fin_r_%=\n"
        "L_fin_d_%=:\n"
        "v_exp_f32 v21, v21\n"
        "v_mfma_f32_32x32x16_bf16 v[32:47], v[152:155], v[180:183], 0\n"
        "buffer_load_dwordx4 v[152:155], v11, s[52:55], s75 offen offset:0 sc1\n"
        "v_exp_f32_e64 v22, v22\n"
        "v_cvt_pk_bf16_f32 v104, v96, v97\n"
        "v_exp_f32_e64 v23, v23\n"
        "v_cvt_pk_bf16_f32 v105, v98, v99\n"
        "v_exp_f32 v24, v24\n"
        "s_add_u32 s76, s76, 1\n"
        "s_and_b32 s76, s76, 7\n"
        "s_lshl_b32 s79, s76, 13\n"
        "s_add_u32 m0, s79, s33\n"
        "s_lshr_b32 s68, s76, 1\n"
        "s_lshl_b32 s68, s68, 17\n"
        "s_bitcmp1_b32 s76, 0\n"
        "s_cselect_b32 s69, s13, s12\n"
        "s_add_u32 s79, s68, s69\n"
        "buffer_load_dwordx4 v254, s[44:47], s79 offen lds\n"
        "v_cvt_pk_bf16_f32 v106, v100, v101\n"
        "v_exp_f32_e64 v25, v25\n"
        "v_cvt_pk_bf16_f32 v107, v102, v103\n"
        "v_exp_f32_e64 v26, v26\n"
        "v_smfmac_f32_32x32x32_bf16 v[184:199], v[156:159], v[16:23], v10\n"
        "v_exp_f32 v27, v27\n"
        "v_exp_f32 v28, v28\n"
        "s_add_u32 m0, m0, 0x10000\n"
        "s_nop 0\n"
        "buffer_load_dwordx4 v253, s[40:43], s79 offen lds\n"
        "v_exp_f32 v29, v29\n"
        "s_waitcnt lgkmcnt(0)\n"
        "v_mfma_f32_32x32x16_bf16 v[64:79], v[104:107], v[216:219], 0\n"
        "v_exp_f32 v30, v30\n"
        "v_exp_f32 v31, v31\n"
        "v_exp_f32 v32, v32\n"
        "v_exp_f32_e64 v33, v33\n"
        "v_smfmac_f32_32x32x32_bf16 v[184:199], v[160:163], v[24:31], v10\n"
        "v_exp_f32 v34, v34\n"
        "v_exp_f32 v35, v35\n"
        "v_exp_f32_e64 v36, v36\n"
        "v_exp_f32 v37, v37\n"
        "s_setprio 0\n"
        "v_mfma_f32_32x32x16_bf16 v[16:31], v[164:167], v[176:179], 0\n"
        "v_exp_f32 v38, v38\n"
        "v_exp_f32 v39, v39\n"
        "v_exp_f32 v40, v40\n"
        "v_exp_f32 v41, v41\n"
        "v_exp_f32_e64 v42, v42\n"
        "v_smfmac_f32_32x32x32_bf16 v[200:215], v[156:159], v[32:39], v10\n"
        "s_mov_b64 exec, s[66:67]\n"
        "buffer_load_dwordx4 v[156:159], v12, s[52:55], s75 offen offset:0 sc1\n"
        "s_mov_b64 exec, -1\n"
        "v_exp_f32_e64 v43, v43\n"
        "v_add_f32 v72, 0x358637bd, v72\n"
        "v_exp_f32 v44, v44\n"
        "v_rcp_f32 v72, v72\n"
        "v_exp_f32_e64 v45, v45\n"
        "v_pk_mul_f32 v[64:65], v[64:65], v[72:73] op_sel_hi:[1,0]\n"
        "v_exp_f32_e64 v46, v46\n"
        "v_pk_mul_f32 v[66:67], v[66:67], v[72:73] op_sel_hi:[1,0]\n"
        "v_exp_f32_e64 v47, v47\n"
        "v_pk_mul_f32 v[68:69], v[68:69], v[72:73] op_sel_hi:[1,0]\n"
        "v_exp_f32_e64 v16, v16\n"
        "v_pk_mul_f32 v[70:71], v[70:71], v[72:73] op_sel_hi:[1,0]\n"
        "v_exp_f32_e64 v17, v17\n"
        "v_smfmac_f32_32x32x32_bf16 v[200:215], v[160:163], v[40:47], v10\n"
        "s_mov_b64 exec, s[66:67]\n"
        "buffer_load_dwordx4 v[160:163], v12, s[52:55], s75 offen offset:512 sc1\n"
        "s_mov_b64 exec, -1\n"
        "v_cvt_pk_bf16_f32 v64, v64, v65\n"
        "v_exp_f32_e64 v18, v18\n"
        "v_cvt_pk_bf16_f32 v65, v66, v67\n"
        "v_exp_f32_e64 v19, v19\n"
        "v_cvt_pk_bf16_f32 v66, v68, v69\n"
        "v_exp_f32_e64 v20, v20\n"
        "v_cvt_pk_bf16_f32 v67, v70, v71\n"
        "v_exp_f32_e64 v21, v21\n"
        "v_mfma_f32_32x32x16_bf16 v[32:47], v[164:167], v[180:183], 0\n"
        "buffer_load_dwordx4 v[164:167], v11, s[52:55], s75 offen offset:2048 sc1\n"
        "v_exp_f32 v22, v22\n"
        "v_exp_f32 v23, v23\n"
        "v_exp_f32 v24, v24\n"
        "v_exp_f32 v25, v25\n"
        "v_exp_f32 v26, v26\n"
        "s_waitcnt vmcnt(17)\n"
        "v_smfmac_f32_32x32x32_bf16 v[184:199], v[168:171], v[16:23], v10\n"
        "v_exp_f32 v27, v27\n"
        "v_exp_f32 v28, v28\n"
        "v_exp_f32 v29, v29\n"
        "v_exp_f32 v30, v30\n"
        "v_exp_f32 v31, v31\n"
        "v_exp_f32 v32, v32\n"
        "v_exp_f32 v33, v33\n"
        "s_waitcnt vmcnt(16)\n"
        "v_smfmac_f32_32x32x32_bf16 v[184:199], v[172:175], v[24:31], v10\n"
        "v_exp_f32 v34, v34\n"
        "v_exp_f32 v35, v35\n"
        "buffer_store_dwordx4 v[64:67], v255, s[52:55], s14 offen sc1\n"
        "v_exp_f32_e64 v36, v36\n"
        "v_exp_f32 v37, v37\n"
        "s_waitcnt vmcnt(16)\n"
        "v_mfma_f32_32x32x16_bf16 v[16:31], v[128:131], v[176:179], 0\n"
        "v_exp_f32 v38, v38\n"
        "v_exp_f32 v39, v39\n"
        "v_exp_f32 v40, v40\n"
        "v_exp_f32 v41, v41\n"
        "v_exp_f32_e64 v42, v42\n"
        "v_smfmac_f32_32x32x32_bf16 v[200:215], v[168:171], v[32:39], v10\n"
        "s_mov_b64 exec, s[66:67]\n"
        "buffer_load_dwordx4 v[168:171], v12, s[52:55], s75 offen offset:2048 sc1\n"
        "s_mov_b64 exec, -1\n"
        "v_exp_f32 v43, v43\n"
        "v_exp_f32 v44, v44\n"
        "v_exp_f32 v45, v45\n"
        "v_exp_f32 v46, v46\n"
        "s_waitcnt vmcnt(5)\n"
        "s_barrier\n"
        "v_exp_f32_e64 v47, v47\n"
        "s_add_u32 s74, s78, 0xc000\n"
        "s_and_b32 s74, s74, 0xffff\n"
        "s_cbranch_vccz L_p640_%=\n"
        "s_setprio 1\n"
        "L_p640_%=:\n"
        "v_exp_f32 v16, v16\n"
        "v_exp_f32 v17, v17\n"
        "v_smfmac_f32_32x32x32_bf16 v[200:215], v[172:175], v[40:47], v10\n"
        "s_mov_b64 exec, s[66:67]\n"
        "buffer_load_dwordx4 v[172:175], v12, s[52:55], s75 offen offset:2560 sc1\n"
        "s_mov_b64 exec, -1\n"
        "v_exp_f32 v18, v18\n"
        "v_exp_f32 v19, v19\n"
        "ds_read_b64_tr_b16 v[64:65], v2 offset:0\n"
        "ds_read_b64_tr_b16 v[66:67], v2 offset:1024\n"
        "v_exp_f32 v20, v20\n"
        "v_exp_f32 v21, v21\n"
        "v_mfma_f32_32x32x16_bf16 v[32:47], v[128:131], v[180:183], 0\n"
        "buffer_load_dwordx4 v[128:131], v11, s[52:55], s74 offen offset:0 sc1\n"
        "v_exp_f32 v22, v22\n"
        "v_exp_f32 v23, v23\n"
        "v_exp_f32 v24, v24\n"
        "v_exp_f32 v25, v25\n"
        "v_exp_f32_e64 v26, v26\n"
        "v_smfmac_f32_32x32x32_bf16 v[184:199], v[132:135], v[16:23], v10\n"
        "v_exp_f32 v27, v27\n"
        "v_exp_f32 v28, v28\n"
        "ds_read_b64_tr_b16 v[68:69], v3 offset:0\n"
        "ds_read_b64_tr_b16 v[70:71], v3 offset:1024\n"
        "v_exp_f32 v29, v29\n"
        "v_exp_f32 v30, v30\n"
        "v_exp_f32 v31, v31\n"
        "v_exp_f32 v32, v32\n"
        "v_exp_f32_e64 v33, v33\n"
        "v_smfmac_f32_32x32x32_bf16 v[184:199], v[136:139], v[24:31], v10\n"
        "v_exp_f32 v34, v34\n"
        "v_exp_f32 v35, v35\n"
        "ds_read_b64_tr_b16 v[72:73], v4 offset:0\n"
        "ds_read_b64_tr_b16 v[74:75], v4 offset:1024\n"
        "v_exp_f32 v36, v36\n"
        "v_exp_f32 v37, v37\n"
        "v_mfma_f32_32x32x16_bf16 v[16:31], v[140:143], v[176:179], 0\n"
        "v_exp_f32 v38, v38\n"
        "v_exp_f32 v39, v39\n"
        "v_exp_f32 v40, v40\n"
        "v_exp_f32 v41, v41\n"
        "v_exp_f32_e64 v42, v42\n"
        "v_smfmac_f32_32x32x32_bf16 v[200:215], v[132:135], v[32:39], v10\n"
        "s_mov_b64 exec, s[66:67]\n"
        "buffer_load_dwordx4 v[132:135], v12, s[52:55], s74 offen offset:0 sc1\n"
        "s_mov_b64 exec, -1\n"
        "v_exp_f32 v43, v43\n"
        "v_exp_f32 v44, v44\n"
        "ds_read_b64_tr_b16 v[76:77], v5 offset:0\n"
        "ds_read_b64_tr_b16 v[78:79], v5 offset:1024\n"
        "v_exp_f32 v45, v45\n"
        "v_exp_f32 v46, v46\n"
        "v_exp_f32 v47, v47\n"
        "s_add_u32 s68, s18, 0\n"
        "s_and_b32 s68, s68, 3\n"
        "s_lshl_b32 s68, s68, 11\n"
        "s_add_u32 s4, s68, 0x700000\n"
        "s_add_u32 s8, s68, 0x500000\n"
        "v_exp_f32 v16, v16\n"
        "v_exp_f32 v17, v17\n"
        "v_smfmac_f32_32x32x32_bf16 v[200:215], v[136:139], v[40:47], v10\n"
        "s_mov_b64 exec, s[66:67]\n"
        "buffer_load_dwordx4 v[136:139], v12, s[52:55], s74 offen offset:512 sc1\n"
        "s_mov_b64 exec, -1\n"
        "v_exp_f32 v18, v18\n"
        "v_exp_f32 v19, v19\n"
        "ds_read_b64_tr_b16 v[80:81], v2 offset:4096\n"
        "ds_read_b64_tr_b16 v[82:83], v2 offset:5120\n"
        "v_exp_f32 v20, v20\n"
        "v_exp_f32 v21, v21\n"
        "v_mfma_f32_32x32x16_bf16 v[32:47], v[140:143], v[180:183], 0\n"
        "buffer_load_dwordx4 v[140:143], v11, s[52:55], s74 offen offset:2048 sc1\n"
        "v_exp_f32 v22, v22\n"
        "v_exp_f32 v23, v23\n"
        "v_exp_f32 v24, v24\n"
        "v_exp_f32 v25, v25\n"
        "v_exp_f32_e64 v26, v26\n"
        "v_smfmac_f32_32x32x32_bf16 v[184:199], v[144:147], v[16:23], v10\n"
        "v_exp_f32_e64 v27, v27\n"
        "v_exp_f32 v28, v28\n"
        "s_waitcnt lgkmcnt(8)\n"
        "ds_read_b64_tr_b16 v[84:85], v3 offset:4096\n"
        "ds_read_b64_tr_b16 v[86:87], v3 offset:5120\n"
        "v_exp_f32 v29, v29\n"
        "v_exp_f32 v30, v30\n"
        "v_exp_f32 v31, v31\n"
        "s_add_u32 s68, s18, 1\n"
        "s_and_b32 s68, s68, 3\n"
        "s_lshl_b32 s68, s68, 11\n"
        "s_add_u32 s5, s68, 0x700000\n"
        "s_add_u32 s9, s68, 0x500000\n"
        "v_exp_f32 v32, v32\n"
        "v_exp_f32 v33, v33\n"
        "v_smfmac_f32_32x32x32_bf16 v[184:199], v[148:151], v[24:31], v10\n"
        "v_exp_f32_e64 v34, v34\n"
        "v_exp_f32 v35, v35\n"
        "s_waitcnt lgkmcnt(8)\n"
        "ds_read_b64_tr_b16 v[88:89], v4 offset:4096\n"
        "ds_read_b64_tr_b16 v[90:91], v4 offset:5120\n"
        "v_exp_f32 v36, v36\n"
        "v_exp_f32 v37, v37\n"
        "v_mfma_f32_32x32x16_bf16 v[16:31], v[152:155], v[176:179], 0\n"
        "v_exp_f32 v38, v38\n"
        "v_exp_f32 v39, v39\n"
        "v_exp_f32 v40, v40\n"
        "v_exp_f32 v41, v41\n"
        "v_exp_f32_e64 v42, v42\n"
        "v_smfmac_f32_32x32x32_bf16 v[200:215], v[144:147], v[32:39], v10\n"
        "s_mov_b64 exec, s[66:67]\n"
        "buffer_load_dwordx4 v[144:147], v12, s[52:55], s74 offen offset:2048 sc1\n"
        "s_mov_b64 exec, -1\n"
        "v_exp_f32_e64 v43, v43\n"
        "v_exp_f32 v44, v44\n"
        "s_waitcnt lgkmcnt(8)\n"
        "ds_read_b64_tr_b16 v[92:93], v5 offset:4096\n"
        "ds_read_b64_tr_b16 v[94:95], v5 offset:5120\n"
        "v_exp_f32 v45, v45\n"
        "v_exp_f32 v46, v46\n"
        "v_exp_f32_e64 v47, v47\n"
        "s_add_u32 s75, s78, 0xd000\n"
        "s_and_b32 s75, s75, 0xffff\n"
        "s_add_u32 s68, s18, 2\n"
        "s_and_b32 s68, s68, 3\n"
        "s_lshl_b32 s68, s68, 11\n"
        "s_add_u32 s6, s68, 0x700000\n"
        "s_add_u32 s10, s68, 0x500000\n"
        "v_exp_f32 v16, v16\n"
        "v_exp_f32_e64 v17, v17\n"
        "v_smfmac_f32_32x32x32_bf16 v[200:215], v[148:151], v[40:47], v10\n"
        "s_mov_b64 exec, s[66:67]\n"
        "buffer_load_dwordx4 v[148:151], v12, s[52:55], s74 offen offset:2560 sc1\n"
        "s_mov_b64 exec, -1\n"
        "v_exp_f32_e64 v18, v18\n"
        "v_exp_f32 v19, v19\n"
        "s_waitcnt lgkmcnt(8)\n"
        "ds_read_b64_tr_b16 v[96:97], v2 offset:8192\n"
        "ds_read_b64_tr_b16 v[98:99], v2 offset:9216\n"
        "v_exp_f32 v20, v20\n"
        "v_exp_f32 v21, v21\n"
        "v_mfma_f32_32x32x16_bf16 v[32:47], v[152:155], v[180:183], 0\n"
        "buffer_load_dwordx4 v[152:155], v11, s[52:55], s75 offen offset:0 sc1\n"
        "v_exp_f32 v22, v22\n"
        "v_exp_f32 v23, v23\n"
        "v_mov_b32 v48, s64\n"
        "v_exp_f32 v24, v24\n"
        "v_mov_b32 v49, s65\n"
        "v_exp_f32 v25, v25\n"
        "v_lshrrev_b32_e64 v50, 7, v255\n"
        "v_exp_f32 v26, v26\n"
        "s_waitcnt vmcnt(12)\n"
        "v_smfmac_f32_32x32x32_bf16 v[184:199], v[156:159], v[16:23], v10\n"
        "v_exp_f32_e64 v27, v27\n"
        "v_exp_f32 v28, v28\n"
        "s_waitcnt lgkmcnt(8)\n"
        "ds_read_b64_tr_b16 v[100:101], v3 offset:8192\n"
        "ds_read_b64_tr_b16 v[102:103], v3 offset:9216\n"
        "v_exp_f32 v29, v29\n"
        "v_exp_f32 v30, v30\n"
        "v_exp_f32 v31, v31\n"
        "s_add_u32 s68, s18, 3\n"
        "s_and_b32 s68, s68, 3\n"
        "s_lshl_b32 s68, s68, 11\n"
        "s_add_u32 s7, s68, 0x700000\n"
        "s_add_u32 s11, s68, 0x500000\n"
        "v_exp_f32_e64 v32, v32\n"
        "v_exp_f32 v33, v33\n"
        "s_waitcnt vmcnt(11)\n"
        "v_smfmac_f32_32x32x32_bf16 v[184:199], v[160:163], v[24:31], v10\n"
        "v_exp_f32_e64 v34, v34\n"
        "v_exp_f32 v35, v35\n"
        "s_waitcnt vmcnt(9)\n"
        "s_mov_b32 s68, 0x2000\n"
        "s_mov_b64 exec, 1\n"
        "buffer_store_dwordx2 v[48:49], v50, s[52:55], s68 offen sc1\n"
        "s_mov_b64 exec, -1\n"
        "s_waitcnt lgkmcnt(8)\n"
        "ds_read_b64_tr_b16 v[104:105], v4 offset:8192\n"
        "ds_read_b64_tr_b16 v[106:107], v4 offset:9216\n"
        "v_exp_f32 v36, v36\n"
        "v_exp_f32 v37, v37\n"
        "s_setprio 0\n"
        "v_mfma_f32_32x32x16_bf16 v[16:31], v[164:167], v[176:179], 0\n"
        "v_exp_f32 v38, v38\n"
        "v_exp_f32 v39, v39\n"
        "v_exp_f32 v40, v40\n"
        "v_exp_f32 v41, v41\n"
        "v_exp_f32_e64 v42, v42\n"
        "v_smfmac_f32_32x32x32_bf16 v[200:215], v[156:159], v[32:39], v10\n"
        "s_mov_b64 exec, s[66:67]\n"
        "buffer_load_dwordx4 v[156:159], v12, s[52:55], s75 offen offset:0 sc1\n"
        "s_mov_b64 exec, -1\n"
        "v_exp_f32_e64 v43, v43\n"
        "v_exp_f32 v44, v44\n"
        "s_waitcnt lgkmcnt(8)\n"
        "ds_read_b64_tr_b16 v[108:109], v5 offset:8192\n"
        "ds_read_b64_tr_b16 v[110:111], v5 offset:9216\n"
        "v_exp_f32 v45, v45\n"
        "v_exp_f32 v46, v46\n"
        "v_exp_f32 v47, v47\n"
        "v_exp_f32 v16, v16\n"
        "v_exp_f32_e64 v17, v17\n"
        "v_smfmac_f32_32x32x32_bf16 v[200:215], v[160:163], v[40:47], v10\n"
        "s_mov_b64 exec, s[66:67]\n"
        "buffer_load_dwordx4 v[160:163], v12, s[52:55], s75 offen offset:512 sc1\n"
        "s_mov_b64 exec, -1\n"
        "v_exp_f32_e64 v18, v18\n"
        "v_exp_f32 v19, v19\n"
        "s_waitcnt lgkmcnt(8)\n"
        "ds_read_b64_tr_b16 v[112:113], v2 offset:12288\n"
        "ds_read_b64_tr_b16 v[114:115], v2 offset:13312\n"
        "v_exp_f32 v20, v20\n"
        "v_exp_f32 v21, v21\n"
        "v_mfma_f32_32x32x16_bf16 v[32:47], v[164:167], v[180:183], 0\n"
        "buffer_load_dwordx4 v[164:167], v11, s[52:55], s75 offen offset:2048 sc1\n"
        "v_exp_f32 v22, v22\n"
        "v_exp_f32 v23, v23\n"
        "v_exp_f32 v24, v24\n"
        "v_exp_f32 v25, v25\n"
        "v_exp_f32 v26, v26\n"
        "s_waitcnt vmcnt(12)\n"
        "v_smfmac_f32_32x32x32_bf16 v[184:199], v[168:171], v[16:23], v10\n"
        "v_exp_f32_e64 v27, v27\n"
        "v_exp_f32 v28, v28\n"
        "s_waitcnt lgkmcnt(8)\n"
        "ds_read_b64_tr_b16 v[116:117], v3 offset:12288\n"
        "ds_read_b64_tr_b16 v[118:119], v3 offset:13312\n"
        "v_exp_f32 v29, v29\n"
        "v_exp_f32 v30, v30\n"
        "v_exp_f32 v31, v31\n"
        "v_exp_f32 v32, v32\n"
        "v_exp_f32 v33, v33\n"
        "s_waitcnt vmcnt(11)\n"
        "v_smfmac_f32_32x32x32_bf16 v[184:199], v[172:175], v[24:31], v10\n"
        "v_exp_f32_e64 v34, v34\n"
        "v_exp_f32 v35, v35\n"
        "s_waitcnt lgkmcnt(8)\n"
        "ds_read_b64_tr_b16 v[120:121], v4 offset:12288\n"
        "ds_read_b64_tr_b16 v[122:123], v4 offset:13312\n"
        "v_exp_f32_e64 v36, v36\n"
        "v_exp_f32 v37, v37\n"
        "s_waitcnt vmcnt(10)\n"
        "v_mfma_f32_32x32x16_bf16 v[16:31], v[128:131], v[176:179], 0\n"
        "v_exp_f32 v38, v38\n"
        "v_exp_f32 v39, v39\n"
        "v_exp_f32 v40, v40\n"
        "v_exp_f32 v41, v41\n"
        "v_exp_f32_e64 v42, v42\n"
        "v_smfmac_f32_32x32x32_bf16 v[200:215], v[168:171], v[32:39], v10\n"
        "s_mov_b64 exec, s[66:67]\n"
        "buffer_load_dwordx4 v[168:171], v12, s[52:55], s75 offen offset:2048 sc1\n"
        "s_mov_b64 exec, -1\n"
        "v_exp_f32_e64 v43, v43\n"
        "v_exp_f32 v44, v44\n"
        "s_waitcnt lgkmcnt(8)\n"
        "ds_read_b64_tr_b16 v[124:125], v5 offset:12288\n"
        "ds_read_b64_tr_b16 v[126:127], v5 offset:13312\n"
        "v_exp_f32 v45, v45\n"
        "v_exp_f32 v46, v46\n"
        "v_exp_f32_e64 v47, v47\n"
        "s_add_u32 s74, s78, 0xe000\n"
        "s_and_b32 s74, s74, 0xffff\n"
        "s_cbranch_vccz L_p768_%=\n"
        "s_setprio 1\n"
        "L_p768_%=:\n"
        "v_exp_f32 v16, v16\n"
        "v_exp_f32 v17, v17\n"
        "v_smfmac_f32_32x32x32_bf16 v[200:215], v[172:175], v[40:47], v10\n"
        "s_mov_b64 exec, s[66:67]\n"
        "buffer_load_dwordx4 v[172:175], v12, s[52:55], s75 offen offset:2560 sc1\n"
        "s_mov_b64 exec, -1\n"
        "v_exp_f32 v18, v18\n"
        "v_exp_f32 v19, v19\n"
        "v_exp_f32 v20, v20\n"
        "v_exp_f32 v21, v21\n"
        "v_mfma_f32_32x32x16_bf16 v[32:47], v[128:131], v[180:183], 0\n"
        "buffer_load_dwordx4 v[128:131], v11, s[52:55], s74 offen offset:0 sc1\n"
        "v_exp_f32 v22, v22\n"
        "v_exp_f32 v23, v23\n"
        "v_exp_f32 v24, v24\n"
        "v_exp_f32 v25, v25\n"
        "v_exp_f32 v26, v26\n"
        "s_waitcnt vmcnt(12)\n"
        "v_smfmac_f32_32x32x32_bf16 v[184:199], v[132:135], v[16:23], v10\n"
        "v_exp_f32 v27, v27\n"
        "v_exp_f32 v28, v28\n"
        "v_exp_f32 v29, v29\n"
        "v_exp_f32 v30, v30\n"
        "v_exp_f32 v31, v31\n"
        "v_exp_f32 v32, v32\n"
        "v_exp_f32 v33, v33\n"
        "s_waitcnt vmcnt(11)\n"
        "v_smfmac_f32_32x32x32_bf16 v[184:199], v[136:139], v[24:31], v10\n"
        "v_exp_f32 v34, v34\n"
        "v_exp_f32 v35, v35\n"
        "v_exp_f32_e64 v36, v36\n"
        "v_exp_f32 v37, v37\n"
        "s_waitcnt vmcnt(10)\n"
        "v_mfma_f32_32x32x16_bf16 v[16:31], v[140:143], v[176:179], 0\n"
        "v_exp_f32 v38, v38\n"
        "v_exp_f32 v39, v39\n"
        "buffer_load_dwordx2 v[252:253], v14, s[52:55], 0 offen sc1\n"
        "v_exp_f32 v40, v40\n"
        "v_exp_f32 v41, v41\n"
        "v_exp_f32_e64 v42, v42\n"
        "v_smfmac_f32_32x32x32_bf16 v[200:215], v[132:135], v[32:39], v10\n"
        "s_mov_b64 exec, s[66:67]\n"
        "buffer_load_dwordx4 v[132:135], v12, s[52:55], s74 offen offset:0 sc1\n"
        "s_mov_b64 exec, -1\n"
        "v_exp_f32 v43, v43\n"
        "v_exp_f32 v44, v44\n"
        "v_exp_f32 v45, v45\n"
        "v_exp_f32 v46, v46\n"
        "v_exp_f32 v47, v47\n"
        "v_exp_f32 v16, v16\n"
        "v_exp_f32_e64 v17, v17\n"
        "v_smfmac_f32_32x32x32_bf16 v[200:215], v[136:139], v[40:47], v10\n"
        "s_mov_b64 exec, s[66:67]\n"
        "buffer_load_dwordx4 v[136:139], v12, s[52:55], s74 offen offset:512 sc1\n"
        "s_mov_b64 exec, -1\n"
        "v_exp_f32 v18, v18\n"
        "v_exp_f32 v19, v19\n"
        "v_exp_f32 v20, v20\n"
        "v_exp_f32 v21, v21\n"
        "v_mfma_f32_32x32x16_bf16 v[32:47], v[140:143], v[180:183], 0\n"
        "buffer_load_dwordx4 v[140:143], v11, s[52:55], s74 offen offset:2048 sc1\n"
        "v_exp_f32 v22, v22\n"
        "v_exp_f32 v23, v23\n"
        "v_exp_f32 v24, v24\n"
        "v_exp_f32 v25, v25\n"
        "v_exp_f32 v26, v26\n"
        "s_waitcnt vmcnt(13)\n"
        "v_smfmac_f32_32x32x32_bf16 v[184:199], v[144:147], v[16:23], v10\n"
        "v_exp_f32 v27, v27\n"
        "v_exp_f32 v28, v28\n"
        "v_exp_f32 v29, v29\n"
        "v_exp_f32 v30, v30\n"
        "v_exp_f32 v31, v31\n"
        "v_exp_f32 v32, v32\n"
        "v_exp_f32 v33, v33\n"
        "s_waitcnt vmcnt(12)\n"
        "v_smfmac_f32_32x32x32_bf16 v[184:199], v[148:151], v[24:31], v10\n"
        "v_exp_f32 v34, v34\n"
        "v_exp_f32 v35, v35\n"
        "v_exp_f32_e64 v36, v36\n"
        "v_exp_f32 v37, v37\n"
        "s_waitcnt vmcnt(11)\n"
        "v_mfma_f32_32x32x16_bf16 v[16:31], v[152:155], v[176:179], 0\n"
        "v_exp_f32 v38, v38\n"
        "v_exp_f32 v39, v39\n"
        "v_exp_f32 v40, v40\n"
        "v_exp_f32 v41, v41\n"
        "v_exp_f32_e64 v42, v42\n"
        "v_smfmac_f32_32x32x32_bf16 v[200:215], v[144:147], v[32:39], v10\n"
        "s_mov_b64 exec, s[66:67]\n"
        "buffer_load_dwordx4 v[144:147], v12, s[52:55], s74 offen offset:2048 sc1\n"
        "s_mov_b64 exec, -1\n"
        "v_exp_f32 v43, v43\n"
        "v_exp_f32 v44, v44\n"
        "v_exp_f32 v45, v45\n"
        "v_exp_f32 v46, v46\n"
        "v_exp_f32_e64 v47, v47\n"
        "s_add_u32 s75, s78, 0xf000\n"
        "s_and_b32 s75, s75, 0xffff\n"
        "v_exp_f32 v16, v16\n"
        "v_exp_f32 v17, v17\n"
        "v_smfmac_f32_32x32x32_bf16 v[200:215], v[148:151], v[40:47], v10\n"
        "s_mov_b64 exec, s[66:67]\n"
        "buffer_load_dwordx4 v[148:151], v12, s[52:55], s74 offen offset:2560 sc1\n"
        "s_mov_b64 exec, -1\n"
        "v_exp_f32 v18, v18\n"
        "v_exp_f32 v19, v19\n"
        "v_exp_f32 v20, v20\n"
        "v_exp_f32 v21, v21\n"
        "v_mfma_f32_32x32x16_bf16 v[32:47], v[152:155], v[180:183], 0\n"
        "buffer_load_dwordx4 v[152:155], v11, s[52:55], s75 offen offset:0 sc1\n"
        "v_exp_f32_e64 v22, v22\n"
        "v_exp_f32 v23, v23\n"
        "s_waitcnt vmcnt(6)\n"
        "v_cmp_ne_u64 s[68:69], v[252:253], s[64:65]\n"
        "s_nop 0\n"
        "s_cmp_lg_u64 s[68:69], 0\n"
        "s_cbranch_scc1 L_f2_r_%=\n"
        "L_f2_d_%=:\n"
        "v_exp_f32 v24, v24\n"
        "v_exp_f32 v25, v25\n"
        "v_exp_f32 v26, v26\n"
        "v_smfmac_f32_32x32x32_bf16 v[184:199], v[156:159], v[16:23], v10\n"
        "v_exp_f32 v27, v27\n"
        "v_exp_f32 v28, v28\n"
        "v_exp_f32_e64 v29, v29\n"
        "buffer_load_dwordx4 v[220:223], v6, s[52:55], s4 offen offset:0 sc1\n"
        "v_exp_f32 v30, v30\n"
        "v_exp_f32 v31, v31\n"
        "v_exp_f32 v32, v32\n"
        "v_exp_f32 v33, v33\n"
        "v_smfmac_f32_32x32x32_bf16 v[184:199], v[160:163], v[24:31], v10\n"
        "v_exp_f32 v34, v34\n"
        "v_exp_f32 v35, v35\n"
        "v_exp_f32_e64 v36, v36\n"
        "v_exp_f32 v37, v37\n"
        "s_setprio 0\n"
        "v_mfma_f32_32x32x16_bf16 v[16:31], v[164:167], v[176:179], 0\n"
        "v_exp_f32 v38, v38\n"
        "v_exp_f32 v39, v39\n"
        "v_exp_f32 v40, v40\n"
        "v_exp_f32 v41, v41\n"
        "v_exp_f32_e64 v42, v42\n"
        "v_smfmac_f32_32x32x32_bf16 v[200:215], v[156:159], v[32:39], v10\n"
        "s_mov_b64 exec, s[66:67]\n"
        "buffer_load_dwordx4 v[156:159], v12, s[52:55], s75 offen offset:0 sc1\n"
        "s_mov_b64 exec, -1\n"
        "v_exp_f32 v43, v43\n"
        "v_exp_f32 v44, v44\n"
        "v_exp_f32_e64 v45, v45\n"
        "buffer_load_dwordx4 v[224:227], v6, s[52:55], s4 offen offset:256 sc1\n"
        "v_exp_f32 v46, v46\n"
        "v_exp_f32 v47, v47\n"
        "v_exp_f32 v16, v16\n"
        "v_exp_f32 v17, v17\n"
        "v_smfmac_f32_32x32x32_bf16 v[200:215], v[160:163], v[40:47], v10\n"
        "s_mov_b64 exec, s[66:67]\n"
        "buffer_load_dwordx4 v[160:163], v12, s[52:55], s75 offen offset:512 sc1\n"
        "s_mov_b64 exec, -1\n"
        "v_exp_f32 v18, v18\n"
        "v_exp_f32 v19, v19\n"
        "v_exp_f32 v20, v20\n"
        "v_exp_f32 v21, v21\n"
        "v_mfma_f32_32x32x16_bf16 v[32:47], v[164:167], v[180:183], 0\n"
        "buffer_load_dwordx4 v[164:167], v11, s[52:55], s75 offen offset:2048 sc1\n"
        "v_exp_f32 v22, v22\n"
        "v_exp_f32 v23, v23\n"
        "v_exp_f32 v24, v24\n"
        "v_exp_f32 v25, v25\n"
        "v_exp_f32_e64 v26, v26\n"
        "v_smfmac_f32_32x32x32_bf16 v[184:199], v[168:171], v[16:23], v10\n"
        "v_exp_f32 v27, v27\n"
        "v_exp_f32 v28, v28\n"
        "v_exp_f32_e64 v29, v29\n"
        "buffer_load_dwordx4 v[228:231], v6, s[52:55], s5 offen offset:0 sc1\n"
        "v_exp_f32 v30, v30\n"
        "v_exp_f32 v31, v31\n"
        "v_exp_f32 v32, v32\n"
        "v_exp_f32 v33, v33\n"
        "v_smfmac_f32_32x32x32_bf16 v[184:199], v[172:175], v[24:31], v10\n"
        "v_exp_f32 v34, v34\n"
        "v_exp_f32 v35, v35\n"
        "v_exp_f32 v36, v36\n"
        "v_exp_f32 v37, v37\n"
        "v_mfma_f32_32x32x16_bf16 v[16:31], v[128:131], v[176:179], 0\n"
        "v_exp_f32 v38, v38\n"
        "v_exp_f32 v39, v39\n"
        "v_exp_f32 v40, v40\n"
        "v_exp_f32 v41, v41\n"
        "v_exp_f32_e64 v42, v42\n"
        "v_smfmac_f32_32x32x32_bf16 v[200:215], v[168:171], v[32:39], v10\n"
        "s_mov_b64 exec, s[66:67]\n"
        "buffer_load_dwordx4 v[168:171], v12, s[52:55], s75 offen offset:2048 sc1\n"
        "s_mov_b64 exec, -1\n"
        "v_exp_f32 v43, v43\n"
        "v_exp_f32 v44, v44\n"
        "v_exp_f32_e64 v45, v45\n"
        "buffer_load_dwordx4 v[232:235], v6, s[52:55], s5 offen offset:256 sc1\n"
        "v_exp_f32_e64 v46, v46\n"
        "v_add_u32 v2, 0x10000, v2\n"
        "v_add_u32 v3, 0x10000, v3\n"
        "v_add_u32 v4, 0x10000, v4\n"
        "v_add_u32 v5, 0x10000, v5\n"
        "v_exp_f32 v47, v47\n"
        "s_cbranch_vccz L_p896_%=\n"
        "s_setprio 1\n"
        "L_p896_%=:\n"
        "s_waitcnt lgkmcnt(8)\n"
        "ds_read_b64_tr_b16 v[48:49], v2 offset:0\n"
        "ds_read_b64_tr_b16 v[50:51], v2 offset:1024\n"
        "v_exp_f32 v16, v16\n"
        "v_exp_f32 v17, v17\n"
        "v_smfmac_f32_32x32x32_bf16 v[200:215], v[172:175], v[40:47], v10\n"
        "s_mov_b64 exec, s[66:67]\n"
        "buffer_load_dwordx4 v[172:175], v12, s[52:55], s75 offen offset:2560 sc1\n"
        "s_mov_b64 exec, -1\n"
        "v_exp_f32_e64 v18, v18\n"
        "v_exp_f32 v19, v19\n"
        "s_waitcnt lgkmcnt(8)\n"
        "ds_read_b64_tr_b16 v[52:53], v3 offset:0\n"
        "ds_read_b64_tr_b16 v[54:55], v3 offset:1024\n"
        "v_exp_f32 v20, v20\n"
        "v_exp_f32 v21, v21\n"
        "v_mfma_f32_32x32x16_bf16 v[32:47], v[128:131], v[180:183], 0\n"
        "v_exp_f32 v22, v22\n"
        "v_exp_f32 v23, v23\n"
        "v_exp_f32 v24, v24\n"
        "s_waitcnt lgkmcnt(8)\n"
        "ds_read_b64_tr_b16 v[56:57], v4 offset:0\n"
        "ds_read_b64_tr_b16 v[58:59], v4 offset:1024\n"
        "v_exp_f32_e64 v25, v25\n"
        "v_exp_f32 v26, v26\n"
        "s_waitcnt vmcnt(14)\n"
        "v_smfmac_f32_32x32x32_bf16 v[184:199], v[132:135], v[16:23], v10\n"
        "v_exp_f32 v27, v27\n"
        "v_exp_f32 v28, v28\n"
        "v_exp_f32_e64 v29, v29\n"
        "buffer_load_dwordx4 v[236:239], v6, s[52:55], s6 offen offset:0 sc1\n"
        "v_exp_f32 v30, v30\n"
        "v_exp_f32 v31, v31\n"
        "v_exp_f32_e64 v32, v32\n"
        "v_exp_f32 v33, v33\n"
        "s_waitcnt vmcnt(14)\n"
        "v_smfmac_f32_32x32x32_bf16 v[184:199], v[136:139], v[24:31], v10\n"
        "v_exp_f32_e64 v34, v34\n"
        "v_exp_f32 v35, v35\n"
        "s_waitcnt lgkmcnt(8)\n"
        "ds_read_b64_tr_b16 v[60:61], v5 offset:0\n"
        "ds_read_b64_tr_b16 v[62:63], v5 offset:1024\n"
        "v_exp_f32_e64 v36, v36\n"
        "v_exp_f32 v37, v37\n"
        "s_waitcnt vmcnt(13)\n"
        "v_mfma_f32_32x32x16_bf16 v[16:31], v[140:143], v[176:179], 0\n"
        "v_exp_f32 v38, v38\n"
        "v_exp_f32 v39, v39\n"
        "v_exp_f32 v40, v40\n"
        "s_waitcnt lgkmcnt(8)\n"
        "ds_read_b64_tr_b16 v[128:129], v2 offset:4096\n"
        "ds_read_b64_tr_b16 v[130:131], v2 offset:5120\n"
        "v_exp_f32 v41, v41\n"
        "v_exp_f32 v42, v42\n"
        "v_smfmac_f32_32x32x32_bf16 v[200:215], v[132:135], v[32:39], v10\n"
        "v_exp_f32_e64 v43, v43\n"
        "v_exp_f32 v44, v44\n"
        "s_waitcnt lgkmcnt(8)\n"
        "ds_read_b64_tr_b16 v[132:133], v3 offset:4096\n"
        "ds_read_b64_tr_b16 v[134:135], v3 offset:5120\n"
        "v_exp_f32_e64 v45, v45\n"
        "buffer_load_dwordx4 v[240:243], v6, s[52:55], s6 offen offset:256 sc1\n"
        "v_exp_f32 v46, v46\n"
        "v_exp_f32 v47, v47\n"
        "v_exp_f32 v16, v16\n"
        "v_exp_f32 v17, v17\n"
        "v_smfmac_f32_32x32x32_bf16 v[200:215], v[136:139], v[40:47], v10\n"
        "v_exp_f32_e64 v18, v18\n"
        "v_exp_f32 v19, v19\n"
        "s_waitcnt lgkmcnt(8)\n"
        "ds_read_b64_tr_b16 v[136:137], v4 offset:4096\n"
        "ds_read_b64_tr_b16 v[138:139], v4 offset:5120\n"
        "v_exp_f32 v20, v20\n"
        "v_exp_f32 v21, v21\n"
        "v_mfma_f32_32x32x16_bf16 v[32:47], v[140:143], v[180:183], 0\n"
        "v_exp_f32 v22, v22\n"
        "v_exp_f32 v23, v23\n"
        "v_exp_f32 v24, v24\n"
        "s_waitcnt lgkmcnt(8)\n"
        "ds_read_b64_tr_b16 v[140:141], v5 offset:4096\n"
        "ds_read_b64_tr_b16 v[142:143], v5 offset:5120\n"
        "v_exp_f32_e64 v25, v25\n"
        "v_exp_f32 v26, v26\n"
        "s_waitcnt vmcnt(13)\n"
        "v_smfmac_f32_32x32x32_bf16 v[184:199], v[144:147], v[16:23], v10\n"
        "v_exp_f32 v27, v27\n"
        "v_exp_f32 v28, v28\n"
        "v_exp_f32_e64 v29, v29\n"
        "buffer_load_dwordx4 v[244:247], v6, s[52:55], s7 offen offset:0 sc1\n"
        "v_exp_f32 v30, v30\n"
        "v_exp_f32 v31, v31\n"
        "v_exp_f32_e64 v32, v32\n"
        "v_exp_f32 v33, v33\n"
        "s_waitcnt vmcnt(13)\n"
        "v_smfmac_f32_32x32x32_bf16 v[184:199], v[148:151], v[24:31], v10\n"
        "v_exp_f32 v34, v34\n"
        "v_exp_f32 v35, v35\n"
        "v_exp_f32_e64 v36, v36\n"
        "v_exp_f32 v37, v37\n"
        "s_waitcnt vmcnt(12)\n"
        "v_mfma_f32_32x32x16_bf16 v[16:31], v[152:155], v[176:179], 0\n"
        "v_exp_f32 v38, v38\n"
        "v_exp_f32 v39, v39\n"
        "v_exp_f32 v40, v40\n"
        "v_exp_f32 v41, v41\n"
        "v_exp_f32_e64 v42, v42\n"
        "v_smfmac_f32_32x32x32_bf16 v[200:215], v[144:147], v[32:39], v10\n"
        "v_exp_f32_e64 v43, v43\n"
        "v_exp_f32 v44, v44\n"
        "s_waitcnt lgkmcnt(8)\n"
        "ds_read_b64_tr_b16 v[144:145], v2 offset:8192\n"
        "ds_read_b64_tr_b16 v[146:147], v2 offset:9216\n"
        "v_exp_f32_e64 v45, v45\n"
        "buffer_load_dwordx4 v[248:251], v6, s[52:55], s7 offen offset:256 sc1\n"
        "v_exp_f32 v46, v46\n"
        "v_exp_f32 v47, v47\n"
        "v_exp_f32 v16, v16\n"
        "v_exp_f32 v17, v17\n"
        "v_smfmac_f32_32x32x32_bf16 v[200:215], v[148:151], v[40:47], v10\n"
        "v_exp_f32_e64 v18, v18\n"
        "v_exp_f32 v19, v19\n"
        "s_waitcnt lgkmcnt(8)\n"
        "ds_read_b64_tr_b16 v[148:149], v3 offset:8192\n"
        "ds_read_b64_tr_b16 v[150:151], v3 offset:9216\n"
        "v_exp_f32 v20, v20\n"
        "v_exp_f32 v21, v21\n"
        "v_mfma_f32_32x32x16_bf16 v[32:47], v[152:155], v[180:183], 0\n"
        "v_exp_f32 v22, v22\n"
        "v_exp_f32 v23, v23\n"
        "v_exp_f32 v24, v24\n"
        "s_waitcnt lgkmcnt(8)\n"
        "ds_read_b64_tr_b16 v[152:153], v4 offset:8192\n"
        "ds_read_b64_tr_b16 v[154:155], v4 offset:9216\n"
        "v_exp_f32_e64 v25, v25\n"
        "v_exp_f32 v26, v26\n"
        "s_waitcnt vmcnt(11)\n"
        "v_smfmac_f32_32x32x32_bf16 v[184:199], v[156:159], v[16:23], v10\n"
        "v_exp_f32 v27, v27\n"
        "v_exp_f32 v28, v28\n"
        "v_exp_f32 v29, v29\n"
        "v_exp_f32 v30, v30\n"
        "v_exp_f32 v31, v31\n"
        "v_exp_f32 v32, v32\n"
        "v_exp_f32 v33, v33\n"
        "s_waitcnt vmcnt(9)\n"
        "v_smfmac_f32_32x32x32_bf16 v[184:199], v[160:163], v[24:31], v10\n"
        "v_exp_f32 v34, v34\n"
        "v_exp_f32 v35, v35\n"
        "v_exp_f32 v36, v36\n"
        "v_exp_f32 v37, v37\n"
        "s_setprio 0\n"
        "s_waitcnt vmcnt(8)\n"
        "v_mfma_f32_32x32x16_bf16 v[16:31], v[164:167], v[176:179], 0\n"
        "v_exp_f32 v38, v38\n"
        "v_exp_f32 v39, v39\n"
        "v_exp_f32 v40, v40\n"
        "v_exp_f32 v41, v41\n"
        "v_exp_f32_e64 v42, v42\n"
        "v_smfmac_f32_32x32x32_bf16 v[200:215], v[156:159], v[32:39], v10\n"
        "v_exp_f32_e64 v43, v43\n"
        "v_exp_f32 v44, v44\n"
        "s_waitcnt lgkmcnt(8)\n"
        "ds_read_b64_tr_b16 v[156:157], v5 offset:8192\n"
        "ds_read_b64_tr_b16 v[158:159], v5 offset:9216\n"
        "v_exp_f32 v45, v45\n"
        "v_exp_f32 v46, v46\n"
        "v_exp_f32 v47, v47\n"
        "v_exp_f32 v16, v16\n"
        "v_exp_f32_e64 v17, v17\n"
        "v_smfmac_f32_32x32x32_bf16 v[200:215], v[160:163], v[40:47], v10\n"
        "v_exp_f32_e64 v18, v18\n"
        "v_exp_f32 v19, v19\n"
        "s_waitcnt lgkmcnt(8)\n"
        "ds_read_b64_tr_b16 v[160:161], v2 offset:12288\n"
        "ds_read_b64_tr_b16 v[162:163], v2 offset:13312\n"
        "v_exp_f32 v20, v20\n"
        "v_exp_f32 v21, v21\n"
        "v_mfma_f32_32x32x16_bf16 v[32:47], v[164:167], v[180:183], 0\n"
        "v_exp_f32 v22, v22\n"
        "v_exp_f32 v23, v23\n"
        "v_exp_f32 v24, v24\n"
        "s_waitcnt lgkmcnt(8)\n"
        "ds_read_b64_tr_b16 v[164:165], v3 offset:12288\n"
        "ds_read_b64_tr_b16 v[166:167], v3 offset:13312\n"
        "v_exp_f32_e64 v25, v25\n"
        "v_exp_f32 v26, v26\n"
        "s_waitcnt vmcnt(6)\n"
        "v_smfmac_f32_32x32x32_bf16 v[184:199], v[168:171], v[16:23], v10\n"
        "v_exp_f32 v27, v27\n"
        "v_exp_f32 v28, v28\n"
        "v_exp_f32 v29, v29\n"
        "v_exp_f32 v30, v30\n"
        "v_exp_f32 v31, v31\n"
        "v_exp_f32 v32, v32\n"
        "v_exp_f32 v33, v33\n"
        "s_waitcnt vmcnt(4)\n"
        "v_smfmac_f32_32x32x32_bf16 v[184:199], v[172:175], v[24:31], v10\n"
        "v_exp_f32 v34, v34\n"
        "v_exp_f32 v35, v35\n"
        "v_exp_f32 v36, v36\n"
        "v_exp_f32 v37, v37\n"
        "v_exp_f32 v38, v38\n"
        "v_exp_f32 v39, v39\n"
        "v_exp_f32 v40, v40\n"
        "v_exp_f32 v41, v41\n"
        "v_exp_f32_e64 v42, v42\n"
        "v_smfmac_f32_32x32x32_bf16 v[200:215], v[168:171], v[32:39], v10\n"
        "v_exp_f32_e64 v43, v43\n"
        "v_exp_f32 v44, v44\n"
        "s_waitcnt lgkmcnt(8)\n"
        "ds_read_b64_tr_b16 v[168:169], v4 offset:12288\n"
        "ds_read_b64_tr_b16 v[170:171], v4 offset:13312\n"
        "ds_write_b128 v7, v[184:187]\n"
        "v_exp_f32_e64 v45, v45\n"
        "ds_write_b128 v7, v[188:191] offset:1024\n"
        "v_exp_f32_e64 v46, v46\n"
        "ds_write_b32 v7, v192 offset:2048\n"
        "v_exp_f32 v47, v47\n"
        "s_setprio 0\n"
        "s_waitcnt lgkmcnt(0)\n"
        "s_barrier\n"
        "v_smfmac_f32_32x32x32_bf16 v[200:215], v[172:175], v[40:47], v10\n"
        "ds_read_b128 v[16:19], v8\n"
        "ds_read_b128 v[20:23], v8 offset:1024\n"
        "ds_read_b32 v24, v8 offset:2048\n"
        "ds_read_b64_tr_b16 v[172:173], v5 offset:12288\n"
        "ds_read_b64_tr_b16 v[174:175], v5 offset:13312\n"
        "s_mov_b32 s70, 0x33800000\n"
        "s_mov_b32 s71, 0x5f800000\n"
        "s_waitcnt lgkmcnt(2)\n"
        "s_nop 3\n"
        "v_add_f32_e64 v24, v24, v208\n"
        "v_pk_add_f32 v[16:17], v[16:17], v[200:201]\n"
        "v_pk_add_f32 v[18:19], v[18:19], v[202:203]\n"
        "v_pk_add_f32 v[20:21], v[20:21], v[204:205]\n"
        "v_pk_add_f32 v[22:23], v[22:23], v[206:207]\n"
        "v_rcp_f32_e64 v30, v24\n"
        "v_cmp_nle_f32 s[68:69], s70, v24\n"
        "v_cmp_nge_f32 vcc, s71, v24\n"
        "v_pk_add_f32 v[26:27], v[16:17], v[18:19]\n"
        "v_pk_add_f32 v[28:29], v[20:21], v[22:23]\n"
        "s_or_b64 s[68:69], s[68:69], vcc\n"
        "v_pk_add_f32 v[26:27], v[26:27], v[28:29]\n"
        "s_mov_b32 s70, 0x207\n"
        "v_add_f32_e64 v26, v26, v27\n"
        "v_cmp_class_f32 vcc, v26, s70\n"
        "s_nop 0\n"
        "s_or_b64 s[68:69], s[68:69], vcc\n"
        "s_cmp_lg_u64 s[68:69], 0\n"
        "s_cbranch_scc1 L_SLOW_%=\n"
        "L_NORM_%=:\n"
        "v_pk_mul_f32 v[16:17], v[16:17], v[30:31] op_sel_hi:[1,0]\n"
        "v_pk_mul_f32 v[18:19], v[18:19], v[30:31] op_sel_hi:[1,0]\n"
        "v_pk_mul_f32 v[20:21], v[20:21], v[30:31] op_sel_hi:[1,0]\n"
        "v_pk_mul_f32 v[22:23], v[22:23], v[30:31] op_sel_hi:[1,0]\n"
        "v_cvt_pk_bf16_f32 v176, v16, v17\n"
        "v_cvt_pk_bf16_f32 v177, v18, v19\n"
        "v_cvt_pk_bf16_f32 v178, v20, v21\n"
        "v_cvt_pk_bf16_f32 v179, v22, v23\n"
        "v_min_u32 v176, -2, v176\n"
        "v_min_u32 v177, -2, v177\n"
        "v_min_u32 v178, -2, v178\n"
        "v_min_u32 v179, -2, v179\n"
        "s_mov_b32 s68, 0x500000\n"
        "buffer_store_dwordx4 v[176:179], v255, s[52:55], s68 offen sc1\n"
        "s_waitcnt vmcnt(1)\n"
        "v_mfma_f32_16x16x32_bf16 v[184:187], v[64:67], v[220:223], 0\n"
        "s_lshl_b32 s68, s60, 3\n"
        "v_mfma_f32_16x16x32_bf16 v[200:203], v[64:67], v[224:227], 0\n"
        "v_lshrrev_b32_e64 v216, 3, v1\n"
        "v_mfma_f32_16x16x32_bf16 v[184:187], v[80:83], v[228:231], v[184:187]\n"
        "v_and_b32_e64 v217, 7, v1\n"
        "v_mfma_f32_16x16x32_bf16 v[200:203], v[80:83], v[232:235], v[200:203]\n"
        "v_lshlrev_b32_e64 v218, 1, v217\n"
        "v_mfma_f32_16x16x32_bf16 v[184:187], v[96:99], v[236:239], v[184:187]\n"
        "v_xor_b32_e64 v218, v218, v216\n"
        "v_mfma_f32_16x16x32_bf16 v[200:203], v[96:99], v[240:243], v[200:203]\n"
        "v_add_u32_e64 v216, s68, v216\n"
        "v_mfma_f32_16x16x32_bf16 v[184:187], v[112:115], v[244:247], v[184:187]\n"
        "v_lshlrev_b32_e64 v219, 8, v216\n"
        "v_mfma_f32_16x16x32_bf16 v[200:203], v[112:115], v[248:251], v[200:203]\n"
        "v_lshl_add_u32 v180, v218, 4, v219\n"
        "v_mfma_f32_16x16x32_bf16 v[188:191], v[68:71], v[220:223], 0\n"
        "v_xor_b32_e64 v181, 16, v180\n"
        "v_mfma_f32_16x16x32_bf16 v[204:207], v[68:71], v[224:227], 0\n"
        "s_lshl_b32 s68, s61, 17\n"
        "v_mfma_f32_16x16x32_bf16 v[188:191], v[84:87], v[228:231], v[188:191]\n"
        "s_xor_b32 s69, s73, 384\n"
        "v_mfma_f32_16x16x32_bf16 v[204:207], v[84:87], v[232:235], v[204:207]\n"
        "s_add_u32 s68, s68, s69\n"
        "v_mfma_f32_16x16x32_bf16 v[188:191], v[100:103], v[236:239], v[188:191]\n"
        "v_lshl_add_u32 v217, v217, 4, s68\n"
        "v_mfma_f32_16x16x32_bf16 v[204:207], v[100:103], v[240:243], v[204:207]\n"
        "v_lshl_add_u32 v182, v216, 11, v217\n"
        "v_mfma_f32_16x16x32_bf16 v[188:191], v[116:119], v[244:247], v[188:191]\n"
        "ds_write_b128 v9, v[184:187] offset:0\n"
        "v_mfma_f32_16x16x32_bf16 v[204:207], v[116:119], v[248:251], v[204:207]\n"
        "ds_write_b128 v9, v[200:203] offset:4096\n"
        "v_mfma_f32_16x16x32_bf16 v[192:195], v[72:75], v[220:223], 0\n"
        "v_mfma_f32_16x16x32_bf16 v[208:211], v[72:75], v[224:227], 0\n"
        "v_mfma_f32_16x16x32_bf16 v[192:195], v[88:91], v[228:231], v[192:195]\n"
        "v_mfma_f32_16x16x32_bf16 v[208:211], v[88:91], v[232:235], v[208:211]\n"
        "v_mfma_f32_16x16x32_bf16 v[192:195], v[104:107], v[236:239], v[192:195]\n"
        "v_mfma_f32_16x16x32_bf16 v[208:211], v[104:107], v[240:243], v[208:211]\n"
        "v_mfma_f32_16x16x32_bf16 v[192:195], v[120:123], v[244:247], v[192:195]\n"
        "ds_write_b128 v15, v[188:191] offset:0\n"
        "v_mfma_f32_16x16x32_bf16 v[208:211], v[120:123], v[248:251], v[208:211]\n"
        "ds_write_b128 v15, v[204:207] offset:4096\n"
        "v_mfma_f32_16x16x32_bf16 v[196:199], v[76:79], v[220:223], 0\n"
        "v_mfma_f32_16x16x32_bf16 v[212:215], v[76:79], v[224:227], 0\n"
        "v_mfma_f32_16x16x32_bf16 v[196:199], v[92:95], v[228:231], v[196:199]\n"
        "v_mfma_f32_16x16x32_bf16 v[212:215], v[92:95], v[232:235], v[212:215]\n"
        "v_mfma_f32_16x16x32_bf16 v[196:199], v[108:111], v[236:239], v[196:199]\n"
        "v_mfma_f32_16x16x32_bf16 v[212:215], v[108:111], v[240:243], v[212:215]\n"
        "v_mfma_f32_16x16x32_bf16 v[196:199], v[124:127], v[244:247], v[196:199]\n"
        "ds_write_b128 v9, v[192:195] offset:128\n"
        "v_mfma_f32_16x16x32_bf16 v[212:215], v[124:127], v[248:251], v[212:215]\n"
        "ds_write_b128 v9, v[208:211] offset:4224\n"
        "s_nop 4\n"
        "ds_write_b128 v15, v[196:199] offset:128\n"
        "s_nop 0\n"
        "ds_write_b128 v15, v[212:215] offset:4224\n"
        "buffer_load_dwordx4 v[16:19], v6, s[52:55], s8 offen offset:0 sc1\n"
        "buffer_load_dwordx4 v[20:23], v6, s[52:55], s8 offen offset:256 sc1\n"
        "s_waitcnt lgkmcnt(0)\n"
        "s_barrier\n"
        "ds_read_b128 v[64:67], v180 offset:0\n"
        "ds_read_b128 v[68:71], v181 offset:0\n"
        "ds_read_b128 v[72:75], v180 offset:16384\n"
        "ds_read_b128 v[76:79], v181 offset:16384\n"
        "ds_read_b128 v[80:83], v180 offset:32768\n"
        "ds_read_b128 v[84:87], v181 offset:32768\n"
        "ds_read_b128 v[88:91], v180 offset:49152\n"
        "ds_read_b128 v[92:95], v181 offset:49152\n"
        "v_add_u32 v9, 0x10000, v9\n"
        "v_add_u32 v15, 0x10000, v15\n"
        "v_add_u32 v180, 0x10000, v180\n"
        "v_add_u32 v181, 0x10000, v181\n"
        "buffer_load_dwordx4 v[24:27], v6, s[52:55], s9 offen offset:0 sc1\n"
        "buffer_load_dwordx4 v[28:31], v6, s[52:55], s9 offen offset:256 sc1\n"
        "s_waitcnt lgkmcnt(4)\n"
        "s_nop 0\n"
        "v_pk_add_f32 v[64:65], v[64:65], v[72:73]\n"
        "v_pk_add_f32 v[66:67], v[66:67], v[74:75]\n"
        "v_pk_add_f32 v[68:69], v[68:69], v[76:77]\n"
        "v_pk_add_f32 v[70:71], v[70:71], v[78:79]\n"
        "buffer_load_dwordx4 v[32:35], v6, s[52:55], s10 offen offset:0 sc1\n"
        "buffer_load_dwordx4 v[36:39], v6, s[52:55], s10 offen offset:256 sc1\n"
        "s_waitcnt lgkmcnt(0)\n"
        "s_nop 0\n"
        "v_pk_add_f32 v[80:81], v[80:81], v[88:89]\n"
        "v_pk_add_f32 v[82:83], v[82:83], v[90:91]\n"
        "v_pk_add_f32 v[84:85], v[84:85], v[92:93]\n"
        "v_pk_add_f32 v[86:87], v[86:87], v[94:95]\n"
        "v_pk_add_f32 v[64:65], v[64:65], v[80:81]\n"
        "v_pk_add_f32 v[66:67], v[66:67], v[82:83]\n"
        "v_pk_add_f32 v[68:69], v[68:69], v[84:85]\n"
        "v_pk_add_f32 v[70:71], v[70:71], v[86:87]\n"
        "v_cvt_pk_bf16_f32 v72, v64, v65\n"
        "v_cvt_pk_bf16_f32 v73, v66, v67\n"
        "v_cvt_pk_bf16_f32 v74, v68, v69\n"
        "v_cvt_pk_bf16_f32 v75, v70, v71\n"
        "buffer_load_dwordx4 v[40:43], v6, s[52:55], s11 offen offset:0 sc1\n"
        "buffer_load_dwordx4 v[44:47], v6, s[52:55], s11 offen offset:256 sc1\n"
        "buffer_store_dwordx4 v[72:75], v182, s[48:51], 0 offen offset:1024 sc0 sc1\n"
        "v_xor_b32 v182, 128, v182\n"
        "s_waitcnt vmcnt(7)\n"
        "v_max3_u32 v183, v16, v17, v18\n"
        "v_max3_u32 v183, v183, v19, v20\n"
        "v_max3_u32 v183, v183, v21, v22\n"
        "v_max_u32 v183, v183, v23\n"
        "v_cmp_eq_u32 s[68:69], -1, v183\n"
        "s_nop 0\n"
        "s_cmp_lg_u64 s[68:69], 0\n"
        "s_cbranch_scc1 L_sa0_r_%=\n"
        "L_sa0_d_%=:\n"
        "v_mfma_f32_16x16x32_bf16 v[184:187], v[48:51], v[16:19], 0\n"
        "v_mfma_f32_16x16x32_bf16 v[200:203], v[48:51], v[20:23], 0\n"
        "v_mfma_f32_16x16x32_bf16 v[188:191], v[52:55], v[16:19], 0\n"
        "v_mfma_f32_16x16x32_bf16 v[204:207], v[52:55], v[20:23], 0\n"
        "s_waitcnt vmcnt(5)\n"
        "v_max3_u32 v183, v24, v25, v26\n"
        "v_max3_u32 v183, v183, v27, v28\n"
        "v_mfma_f32_16x16x32_bf16 v[192:195], v[56:59], v[16:19], 0\n"
        "v_mfma_f32_16x16x32_bf16 v[208:211], v[56:59], v[20:23], 0\n"
        "v_max3_u32 v183, v183, v29, v30\n"
        "v_max_u32_e64 v183, v183, v31\n"
        "v_mfma_f32_16x16x32_bf16 v[196:199], v[60:63], v[16:19], 0\n"
        "v_mfma_f32_16x16x32_bf16 v[212:215], v[60:63], v[20:23], 0\n"
        "v_cmp_eq_u32 s[68:69], -1, v183\n"
        "s_nop 0\n"
        "s_cmp_lg_u64 s[68:69], 0\n"
        "s_cbranch_scc1 L_sa1_r_%=\n"
        "L_sa1_d_%=:\n"
        "v_mfma_f32_16x16x32_bf16 v[184:187], v[128:131], v[24:27], v[184:187]\n"
        "v_mfma_f32_16x16x32_bf16 v[200:203], v[128:131], v[28:31], v[200:203]\n"
        "v_mfma_f32_16x16x32_bf16 v[188:191], v[132:135], v[24:27], v[188:191]\n"
        "v_mfma_f32_16x16x32_bf16 v[204:207], v[132:135], v[28:31], v[204:207]\n"
        "s_waitcnt vmcnt(3)\n"
        "v_max3_u32 v183, v32, v33, v34\n"
        "v_max3_u32 v183, v183, v35, v36\n"
        "v_mfma_f32_16x16x32_bf16 v[192:195], v[136:139], v[24:27], v[192:195]\n"
        "v_mfma_f32_16x16x32_bf16 v[208:211], v[136:139], v[28:31], v[208:211]\n"
        "v_max3_u32 v183, v183, v37, v38\n"
        "v_max_u32_e64 v183, v183, v39\n"
        "v_mfma_f32_16x16x32_bf16 v[196:199], v[140:143], v[24:27], v[196:199]\n"
        "v_mfma_f32_16x16x32_bf16 v[212:215], v[140:143], v[28:31], v[212:215]\n"
        "v_cmp_eq_u32 s[68:69], -1, v183\n"
        "s_nop 0\n"
        "s_cmp_lg_u64 s[68:69], 0\n"
        "s_cbranch_scc1 L_sa2_r_%=\n"
        "L_sa2_d_%=:\n"
        "v_mfma_f32_16x16x32_bf16 v[184:187], v[144:147], v[32:35], v[184:187]\n"
        "v_mfma_f32_16x16x32_bf16 v[200:203], v[144:147], v[36:39], v[200:203]\n"
        "v_mfma_f32_16x16x32_bf16 v[188:191], v[148:151], v[32:35], v[188:191]\n"
        "v_mfma_f32_16x16x32_bf16 v[204:207], v[148:151], v[36:39], v[204:207]\n"
        "s_waitcnt vmcnt(1)\n"
        "v_max3_u32 v183, v40, v41, v42\n"
        "v_max3_u32 v183, v183, v43, v44\n"
        "v_mfma_f32_16x16x32_bf16 v[192:195], v[152:155], v[32:35], v[192:195]\n"
        "v_mfma_f32_16x16x32_bf16 v[208:211], v[152:155], v[36:39], v[208:211]\n"
        "v_max3_u32 v183, v183, v45, v46\n"
        "v_max_u32_e64 v183, v183, v47\n"
        "v_mfma_f32_16x16x32_bf16 v[196:199], v[156:159], v[32:35], v[196:199]\n"
        "v_mfma_f32_16x16x32_bf16 v[212:215], v[156:159], v[36:39], v[212:215]\n"
        "v_cmp_eq_u32 s[68:69], -1, v183\n"
        "s_nop 0\n"
        "s_cmp_lg_u64 s[68:69], 0\n"
        "s_cbranch_scc1 L_sa3_r_%=\n"
        "L_sa3_d_%=:\n"
        "s_nop 0\n"
        "v_mfma_f32_16x16x32_bf16 v[184:187], v[160:163], v[40:43], v[184:187]\n"
        "v_mfma_f32_16x16x32_bf16 v[200:203], v[160:163], v[44:47], v[200:203]\n"
        "v_mfma_f32_16x16x32_bf16 v[188:191], v[164:167], v[40:43], v[188:191]\n"
        "v_mfma_f32_16x16x32_bf16 v[204:207], v[164:167], v[44:47], v[204:207]\n"
        "v_mfma_f32_16x16x32_bf16 v[192:195], v[168:171], v[40:43], v[192:195]\n"
        "v_mfma_f32_16x16x32_bf16 v[208:211], v[168:171], v[44:47], v[208:211]\n"
        "s_nop 2\n"
        "s_nop 0\n"
        "ds_write_b128 v9, v[184:187] offset:0\n"
        "ds_write_b128 v9, v[200:203] offset:4096\n"
        "v_mfma_f32_16x16x32_bf16 v[196:199], v[172:175], v[40:43], v[196:199]\n"
        "v_mfma_f32_16x16x32_bf16 v[212:215], v[172:175], v[44:47], v[212:215]\n"
        "ds_write_b128 v15, v[188:191] offset:0\n"
        "ds_write_b128 v15, v[204:207] offset:4096\n"
        "ds_write_b128 v9, v[192:195] offset:128\n"
        "ds_write_b128 v9, v[208:211] offset:4224\n"
        "s_nop 2\n"
        "ds_write_b128 v15, v[196:199] offset:128\n"
        "ds_write_b128 v15, v[212:215] offset:4224\n"
        "s_waitcnt lgkmcnt(0)\n"
        "s_barrier\n"
        "s_nop 0\n"
        "ds_read_b128 v[64:67], v180 offset:0\n"
        "ds_read_b128 v[68:71], v181 offset:0\n"
        "ds_read_b128 v[72:75], v180 offset:16384\n"
        "ds_read_b128 v[76:79], v181 offset:16384\n"
        "ds_read_b128 v[80:83], v180 offset:32768\n"
        "ds_read_b128 v[84:87], v181 offset:32768\n"
        "ds_read_b128 v[88:91], v180 offset:49152\n"
        "ds_read_b128 v[92:95], v181 offset:49152\n"
        "s_waitcnt lgkmcnt(4)\n"
        "v_pk_add_f32 v[64:65], v[64:65], v[72:73]\n"
        "v_pk_add_f32 v[66:67], v[66:67], v[74:75]\n"
        "v_pk_add_f32 v[68:69], v[68:69], v[76:77]\n"
        "v_pk_add_f32 v[70:71], v[70:71], v[78:79]\n"
        "s_waitcnt lgkmcnt(0)\n"
        "v_pk_add_f32 v[80:81], v[80:81], v[88:89]\n"
        "v_pk_add_f32 v[82:83], v[82:83], v[90:91]\n"
        "v_pk_add_f32 v[84:85], v[84:85], v[92:93]\n"
        "v_pk_add_f32 v[86:87], v[86:87], v[94:95]\n"
        "v_pk_add_f32 v[64:65], v[64:65], v[80:81]\n"
        "v_pk_add_f32 v[66:67], v[66:67], v[82:83]\n"
        "v_pk_add_f32 v[68:69], v[68:69], v[84:85]\n"
        "v_pk_add_f32 v[70:71], v[70:71], v[86:87]\n"
        "v_cvt_pk_bf16_f32 v72, v64, v65\n"
        "v_cvt_pk_bf16_f32 v73, v66, v67\n"
        "v_cvt_pk_bf16_f32 v74, v68, v69\n"
        "v_cvt_pk_bf16_f32 v75, v70, v71\n"
        "buffer_store_dwordx4 v[72:75], v182, s[48:51], 0 offen offset:0 sc0 sc1\n"
        "s_or_b32 s68, s61, s62\n"
        "s_or_b32 s68, s68, s60\n"
        "s_cmp_lg_u32 s68, 0\n"
        "s_cbranch_scc1 L_noep_%=\n"
        "v_mov_b32_e64 v64, s64\n"
        "v_mov_b32 v65, s65\n"
        "s_mov_b64 exec, 1\n"
        "buffer_store_dwordx2 v[64:65], off, s[52:55], 0 sc1\n"
        "s_mov_b64 exec, -1\n"
        "L_noep_%=:\n"
        "s_branch L_END_%=\n"
        "L_fl_r_%=:\n"
        "s_cmp_eq_u32 s72, 0\n"
        "s_cbranch_scc1 L_fl_d_%=\n"
        "s_sub_u32 s72, s72, 1\n"
        "buffer_load_dwordx2 v[252:253], v14, s[52:55], 0 offen sc1\n"
        "s_waitcnt vmcnt(0)\n"
        "v_cmp_ne_u64 s[68:69], v[252:253], s[64:65]\n"
        "s_nop 0\n"
        "s_cmp_lg_u64 s[68:69], 0\n"
        "s_cbranch_scc1 L_fl_r_%=\n"
        "s_branch L_fl_d_%=\n"
        "L_fp_%=:\n"
        "s_lshl_b32 s68, s62, 5\n"
        "s_add_u32 s68, s68, s61\n"
        "s_lshl_b32 s68, s68, 2\n"
        "s_add_u32 s68, s68, s60\n"
        "s_lshl_b32 s68, s68, 3\n"
        "s_add_u32 s68, s68, 0x8000\n"
        "v_mov_b32 v52, s64\n"
        "v_mov_b32 v53, s65\n"
        "s_mov_b64 exec, 1\n"
        "buffer_store_dwordx2 v[52:53], off, s[52:55], s68 sc1\n"
        "s_mov_b64 exec, -1\n"
        "s_branch L_fp_b_%=\n"
        "L_pp_%=:\n"
        "s_lshl_b32 s68, s62, 10\n"
        "s_add_u32 s68, s68, 0x8000\n"
        "v_lshl_add_u32 v56, v1, 3, s68\n"
        "buffer_load_dwordx2 v[52:53], v56, s[52:55], 0 offen sc1\n"
        "buffer_load_dwordx2 v[54:55], v56, s[52:55], 0 offen offset:512 sc1\n"
        "s_branch L_pp_b_%=\n"
        "L_red0_%=:\n"
        "s_lshl_b32 s68, s60, 5\n"
        "s_add_u32 s69, s68, s61\n"
        "s_cmp_ge_u32 s69, 34\n"
        "s_cbranch_scc1 L_red0_b_%=\n"
        "s_mul_i32 s68, s62, 139264\n"
        "s_lshl_b32 s70, s69, 7\n"
        "s_add_u32 s68, s68, s70\n"
        "s_add_u32 s4, s68, 0x900000\n"
        "s_add_u32 s5, s4, 34816\n"
        "s_add_u32 s6, s5, 34816\n"
        "s_add_u32 s7, s6, 34816\n"
        "s_mul_i32 s68, s62, 4352\n"
        "s_add_u32 s68, s68, s70\n"
        "s_add_u32 s68, s68, 0xb00000\n"
        "v_lshrrev_b32 v80, 3, v1\n"
        "v_mul_u32_u24 v80, 4352, v80\n"
        "v_and_b32_e64 v81, 7, v1\n"
        "v_lshl_add_u32 v80, v81, 4, v80\n"
        "v_lshl_add_u32 v81, v81, 4, s68\n"
        "s_mov_b64 exec, 0xff\n"
        "buffer_store_dwordx4 v[48:51], v81, s[52:55], 0 offen sc1\n"
        "s_mov_b64 exec, -1\n"
        "s_branch L_red0_b_%=\n"
        "L_red1_%=:\n"
        "s_lshl_b32 s68, s60, 5\n"
        "s_add_u32 s69, s68, s61\n"
        "s_cmp_ge_u32 s69, 34\n"
        "s_cbranch_scc1 L_red1_b_%=\n"
        "buffer_load_dwordx4 v[64:67], v80, s[52:55], s4 offen sc1\n"
        "buffer_load_dwordx4 v[68:71], v80, s[52:55], s5 offen sc1\n"
        "buffer_load_dwordx4 v[72:75], v80, s[52:55], s6 offen sc1\n"
        "buffer_load_dwordx4 v[76:79], v80, s[52:55], s7 offen sc1\n"
        "s_branch L_red1_b_%=\n"
        "L_red2_%=:\n"
        "s_lshl_b32 s68, s60, 5\n"
        "s_add_u32 s69, s68, s61\n"
        "s_cmp_ge_u32 s69, 34\n"
        "s_cbranch_scc1 L_red2_b_%=\n"
        "s_waitcnt vmcnt(9)\n"
        "v_pk_add_f32 v[64:65], v[64:65], v[68:69]\n"
        "v_pk_add_f32 v[72:73], v[72:73], v[76:77]\n"
        "v_pk_add_f32 v[66:67], v[66:67], v[70:71]\n"
        "v_pk_add_f32 v[74:75], v[74:75], v[78:79]\n"
        "v_pk_add_f32 v[64:65], v[64:65], v[72:73]\n"
        "v_pk_add_f32 v[66:67], v[66:67], v[74:75]\n"
        "s_nop 0\n"
        "v_add_f32_dpp v68, v64, v64 row_ror:8 row_mask:0xf bank_mask:0xf\n"
        "v_add_f32_dpp v69, v65, v65 row_ror:8 row_mask:0xf bank_mask:0xf\n"
        "v_add_f32_dpp v70, v66, v66 row_ror:8 row_mask:0xf bank_mask:0xf\n"
        "v_add_f32_dpp v71, v67, v67 row_ror:8 row_mask:0xf bank_mask:0xf\n"
        "v_mov_b32 v64, v68\n"
        "v_mov_b32 v65, v69\n"
        "v_mov_b32 v66, v70\n"
        "v_mov_b32_e64 v67, v71\n"
        "v_permlane16_swap_b32 v64, v68\n"
        "v_permlane16_swap_b32 v65, v69\n"
        "v_permlane16_swap_b32 v66, v70\n"
        "v_permlane16_swap_b32 v67, v71\n"
        "v_pk_add_f32 v[68:69], v[64:65], v[68:69]\n"
        "v_pk_add_f32 v[70:71], v[66:67], v[70:71]\n"
        "v_mov_b32 v64, v68\n"
        "v_mov_b32 v65, v69\n"
        "v_mov_b32 v66, v70\n"
        "v_mov_b32 v67, v71\n"
        "v_permlane32_swap_b32 v64, v68\n"
        "v_permlane32_swap_b32 v65, v69\n"
        "v_permlane32_swap_b32 v66, v70\n"
        "v_permlane32_swap_b32 v67, v71\n"
        "v_pk_add_f32 v[68:69], v[64:65], v[68:69]\n"
        "v_pk_add_f32 v[70:71], v[66:67], v[70:71]\n"
        "v_min_u32 v68, -2, v68\n"
        "v_min_u32 v69, -2, v69\n"
        "v_min_u32 v70, -2, v70\n"
        "v_min_u32 v71, -2, v71\n"
        "s_mov_b64 exec, 0xff\n"
        "buffer_store_dwordx4 v[68:71], v81, s[52:55], 0 offen sc1\n"
        "s_mov_b64 exec, -1\n"
        "s_branch L_red2_b_%=\n"
        "L_pc_%=:\n"
        "s_waitcnt vmcnt(5)\n"
        "v_cmp_ne_u64 s[68:69], v[52:53], s[64:65]\n"
        "v_cmp_ne_u64 s[70:71], v[54:55], s[64:65]\n"
        "s_nop 0\n"
        "s_or_b64 s[68:69], s[68:69], s[70:71]\n"
        "s_cmp_lg_u64 s[68:69], 0\n"
        "s_cbranch_scc0 L_pc_d_%=\n"
        "L_pc_r_%=:\n"
        "s_cmp_eq_u32 s72, 0\n"
        "s_cbranch_scc1 L_pc_d_%=\n"
        "s_sub_u32 s72, s72, 1\n"
        "buffer_load_dwordx2 v[52:53], v56, s[52:55], 0 offen sc1\n"
        "buffer_load_dwordx2 v[54:55], v56, s[52:55], 0 offen offset:512 sc1\n"
        "s_waitcnt vmcnt(0)\n"
        "v_cmp_ne_u64 s[68:69], v[52:53], s[64:65]\n"
        "v_cmp_ne_u64 s[70:71], v[54:55], s[64:65]\n"
        "s_nop 0\n"
        "s_or_b64 s[68:69], s[68:69], s[70:71]\n"
        "s_cmp_lg_u64 s[68:69], 0\n"
        "s_cbranch_scc1 L_pc_r_%=\n"
        "s_branch L_pc_d_%=\n"
        "L_fin_r_%=:\n"
        "s_cmp_eq_u32 s72, 0\n"
        "s_cbranch_scc1 L_fin_d_%=\n"
        "s_sub_u32 s72, s72, 1\n"
        "buffer_load_dwordx4 v[96:99], v112, s[52:55], 0 offen sc1\n"
        "buffer_load_dwordx4 v[100:103], v112, s[52:55], 0 offen offset:16 sc1\n"
        "s_waitcnt vmcnt(0)\n"
        "v_max3_u32 v113, v96, v97, v98\n"
        "v_max3_u32 v113, v113, v99, v100\n"
        "v_max3_u32 v113, v113, v101, v102\n"
        "v_max_u32_e64 v113, v113, v103\n"
        "v_cmp_eq_u32 s[68:69], -1, v113\n"
        "s_nop 0\n"
        "s_cmp_lg_u64 s[68:69], 0\n"
        "s_cbranch_scc1 L_fin_r_%=\n"
        "s_branch L_fin_d_%=\n"
        "L_f2_r_%=:\n"
        "s_cmp_eq_u32 s72, 0\n"
        "s_cbranch_scc1 L_f2_d_%=\n"
        "s_sub_u32 s72, s72, 1\n"
        "buffer_load_dwordx2 v[252:253], v14, s[52:55], 0 offen sc1\n"
        "s_waitcnt vmcnt(0)\n"
        "v_cmp_ne_u64 s[68:69], v[252:253], s[64:65]\n"
        "s_nop 0\n"
        "s_cmp_lg_u64 s[68:69], 0\n"
        "s_cbranch_scc1 L_f2_r_%=\n"
        "s_branch L_f2_d_%=\n"
        "L_sa0_r_%=:\n"
        "s_cmp_eq_u32 s72, 0\n"
        "s_cbranch_scc1 L_sa0_d_%=\n"
        "s_sub_u32 s72, s72, 1\n"
        "buffer_load_dwordx4 v[16:19], v6, s[52:55], s8 offen offset:0 sc1\n"
        "buffer_load_dwordx4 v[20:23], v6, s[52:55], s8 offen offset:256 sc1\n"
        "s_waitcnt vmcnt(0)\n"
        "v_max3_u32 v183, v16, v17, v18\n"
        "v_max3_u32 v183, v183, v19, v20\n"
        "v_max3_u32 v183, v183, v21, v22\n"
        "v_max_u32_e64 v183, v183, v23\n"
        "v_cmp_eq_u32 s[68:69], -1, v183\n"
        "s_nop 0\n"
        "s_cmp_lg_u64 s[68:69], 0\n"
        "s_cbranch_scc1 L_sa0_r_%=\n"
        "s_branch L_sa0_d_%=\n"
        "L_sa1_r_%=:\n"
        "s_cmp_eq_u32 s72, 0\n"
        "s_cbranch_scc1 L_sa1_d_%=\n"
        "s_sub_u32 s72, s72, 1\n"
        "buffer_load_dwordx4 v[24:27], v6, s[52:55], s9 offen offset:0 sc1\n"
        "buffer_load_dwordx4 v[28:31], v6, s[52:55], s9 offen offset:256 sc1\n"
        "s_waitcnt vmcnt(0)\n"
        "v_max3_u32 v183, v24, v25, v26\n"
        "v_max3_u32 v183, v183, v27, v28\n"
        "v_max3_u32 v183, v183, v29, v30\n"
        "v_max_u32_e64 v183, v183, v31\n"
        "v_cmp_eq_u32 s[68:69], -1, v183\n"
        "s_nop 0\n"
        "s_cmp_lg_u64 s[68:69], 0\n"
        "s_cbranch_scc1 L_sa1_r_%=\n"
        "s_branch L_sa1_d_%=\n"
        "L_sa2_r_%=:\n"
        "s_cmp_eq_u32 s72, 0\n"
        "s_cbranch_scc1 L_sa2_d_%=\n"
        "s_sub_u32 s72, s72, 1\n"
        "buffer_load_dwordx4 v[32:35], v6, s[52:55], s10 offen offset:0 sc1\n"
        "buffer_load_dwordx4 v[36:39], v6, s[52:55], s10 offen offset:256 sc1\n"
        "s_waitcnt vmcnt(0)\n"
        "v_max3_u32 v183, v32, v33, v34\n"
        "v_max3_u32 v183, v183, v35, v36\n"
        "v_max3_u32 v183, v183, v37, v38\n"
        "v_max_u32_e64 v183, v183, v39\n"
        "v_cmp_eq_u32 s[68:69], -1, v183\n"
        "s_nop 0\n"
        "s_cmp_lg_u64 s[68:69], 0\n"
        "s_cbranch_scc1 L_sa2_r_%=\n"
        "s_branch L_sa2_d_%=\n"
        "L_sa3_r_%=:\n"
        "s_cmp_eq_u32 s72, 0\n"
        "s_cbranch_scc1 L_sa3_d_%=\n"
        "s_sub_u32 s72, s72, 1\n"
        "buffer_load_dwordx4 v[40:43], v6, s[52:55], s11 offen offset:0 sc1\n"
        "buffer_load_dwordx4 v[44:47], v6, s[52:55], s11 offen offset:256 sc1\n"
        "s_waitcnt vmcnt(0)\n"
        "v_max3_u32 v183, v40, v41, v42\n"
        "v_max3_u32 v183, v183, v43, v44\n"
        "v_max3_u32 v183, v183, v45, v46\n"
        "v_max_u32_e64 v183, v183, v47\n"
        "v_cmp_eq_u32 s[68:69], -1, v183\n"
        "s_nop 0\n"
        "s_cmp_lg_u64 s[68:69], 0\n"
        "s_cbranch_scc1 L_sa3_r_%=\n"
        "s_branch L_sa3_d_%=\n"
        "L_SLOW_%=:\n"
        "s_lshr_b32 s68, s60, 2\n"
        "s_lshl_b32 s68, s68, 16\n"
        "v_subrev_u32 v11, s68, v11\n"
        "v_subrev_u32 v12, s68, v12\n"
        "s_mov_b32 s70, 0\n"
        "v_mov_b32 v7, 0xff61b1e6\n"
        "v_and_b32 v8, 27, v1\n"
        "v_cmp_eq_u32 vcc, 16, v8\n"
        "v_mov_b32 v8, 0x3f803f80\n"
        "s_nop 0\n"
        "v_cndmask_b32 v8, 0, v8, vcc\n"
        "v_mov_b32 v36, v8\n"
        "v_mov_b32 v37, v8\n"
        "v_mov_b32 v38, v8\n"
        "v_mov_b32 v39, v8\n"
        "v_mov_b32 v40, v8\n"
        "v_mov_b32 v41, v8\n"
        "v_mov_b32 v42, v8\n"
        "v_mov_b32 v43, v8\n"
        "v_mov_b32 v184, 0\n"
        "v_mov_b32 v185, 0\n"
        "v_mov_b32 v186, 0\n"
        "v_mov_b32 v187, 0\n"
        "v_mov_b32 v188, 0\n"
        "v_mov_b32 v189, 0\n"
        "v_mov_b32 v190, 0\n"
        "v_mov_b32 v191, 0\n"
        "v_mov_b32 v192, 0\n"
        "v_mov_b32 v193, 0\n"
        "v_mov_b32 v194, 0\n"
        "v_mov_b32 v195, 0\n"
        "v_mov_b32 v196, 0\n"
        "v_mov_b32 v197, 0\n"
        "v_mov_b32 v198, 0\n"
        "v_mov_b32_e64 v199, 0\n"
        "L_slow1_%=:\n"
        "buffer_load_dwordx4 v[32:35], v11, s[52:55], s70 offen sc1\n"
        "s_mov_b64 exec, s[66:67]\n"
        "buffer_load_dwordx4 v[36:39], v12, s[52:55], s70 offen sc1\n"
        "buffer_load_dwordx4 v[40:43], v12, s[52:55], s70 offen offset:512 sc1\n"
        "s_mov_b64 exec, -1\n"
        "s_waitcnt vmcnt(0)\n"
        "v_mfma_f32_32x32x16_bf16 v[16:31], v[32:35], v[180:183], 0\n"
        "s_nop 11\n"
        "v_max3_f32 v8, v7, v16, v17\n"
        "v_max3_f32 v8, v8, v18, v19\n"
        "v_max3_f32 v8, v8, v20, v21\n"
        "v_max3_f32 v8, v8, v22, v23\n"
        "v_max3_f32 v8, v8, v24, v25\n"
        "v_max3_f32 v8, v8, v26, v27\n"
        "v_max3_f32 v8, v8, v28, v29\n"
        "v_max3_f32 v8, v8, v30, v31\n"
        "v_mov_b32 v44, v8\n"
        "s_nop 1\n"
        "v_permlane32_swap_b32 v8, v44\n"
        "v_max_f32 v8, v8, v44\n"
        "v_sub_f32 v44, v7, v8\n"
        "v_exp_f32 v44, v44\n"
        "v_mov_b32 v7, v8\n"
        "v_sub_f32 v16, v16, v8\n"
        "v_mul_f32 v184, v184, v44\n"
        "v_sub_f32 v17, v17, v8\n"
        "v_mul_f32 v185, v185, v44\n"
        "v_sub_f32 v18, v18, v8\n"
        "v_mul_f32 v186, v186, v44\n"
        "v_sub_f32 v19, v19, v8\n"
        "v_mul_f32 v187, v187, v44\n"
        "v_sub_f32 v20, v20, v8\n"
        "v_mul_f32 v188, v188, v44\n"
        "v_sub_f32 v21, v21, v8\n"
        "v_mul_f32 v189, v189, v44\n"
        "v_sub_f32 v22, v22, v8\n"
        "v_mul_f32 v190, v190, v44\n"
        "v_sub_f32 v23, v23, v8\n"
        "v_mul_f32 v191, v191, v44\n"
        "v_sub_f32 v24, v24, v8\n"
        "v_mul_f32 v192, v192, v44\n"
        "v_sub_f32 v25, v25, v8\n"
        "v_mul_f32 v193, v193, v44\n"
        "v_sub_f32 v26, v26, v8\n"
        "v_mul_f32 v194, v194, v44\n"
        "v_sub_f32 v27, v27, v8\n"
        "v_mul_f32 v195, v195, v44\n"
        "v_sub_f32 v28, v28, v8\n"
        "v_mul_f32 v196, v196, v44\n"
        "v_sub_f32 v29, v29, v8\n"
        "v_mul_f32 v197, v197, v44\n"
        "v_sub_f32 v30, v30, v8\n"
        "v_mul_f32 v198, v198, v44\n"
        "v_sub_f32 v31, v31, v8\n"
        "v_mul_f32 v199, v199, v44\n"
        "v_exp_f32 v16, v16\n"
        "v_exp_f32 v17, v17\n"
        "v_exp_f32 v18, v18\n"
        "v_exp_f32 v19, v19\n"
        "v_exp_f32 v20, v20\n"
        "v_exp_f32 v21, v21\n"
        "v_exp_f32 v22, v22\n"
        "v_exp_f32 v23, v23\n"
        "v_exp_f32 v24, v24\n"
        "v_exp_f32 v25, v25\n"
        "v_exp_f32 v26, v26\n"
        "v_exp_f32 v27, v27\n"
        "v_exp_f32 v28, v28\n"
        "v_exp_f32 v29, v29\n"
        "v_exp_f32 v30, v30\n"
        "v_exp_f32_e64 v31, v31\n"
        "v_smfmac_f32_32x32x32_bf16 v[184:199], v[36:39], v[16:23], v10\n"
        "s_nop 0\n"
        "v_smfmac_f32_32x32x32_bf16 v[184:199], v[40:43], v[24:31], v10\n"
        "s_add_u32 s70, s70, 0x800\n"
        "s_cmp_lt_u32 s70, 0x20000\n"
        "s_cbranch_scc1 L_slow1_%=\n"
        "s_nop 8\n"
        "v_rcp_f32 v30, v192\n"
        "v_mov_b32 v16, v184\n"
        "v_mov_b32 v17, v185\n"
        "v_mov_b32 v18, v186\n"
        "v_mov_b32 v19, v187\n"
        "v_mov_b32 v20, v188\n"
        "v_mov_b32 v21, v189\n"
        "v_mov_b32 v22, v190\n"
        "v_mov_b32 v23, v191\n"
        "s_branch L_NORM_%=\n"
        "L_END_%=:\n"
        :
        : "s"(__builtin_amdgcn_kernarg_segment_ptr()), "s"(blockIdx.x), "v"(threadIdx.x)
        : "v1", "v2", "v3", "v4", "v5", "v6", "v7", "v8", "v9", "v10", "v11", "v12", "v13", "v14", "v15", "v16", "v17", "v18", "v19", "v20", "v21", "v22", "v23", "v24", "v25", "v26", "v27", "v28", "v29", "v30", "v31", "v32", "v33", "v34", "v35", "v36", "v37", "v38", "v39", "v40", "v41", "v42", "v43", "v44", "v45", "v46", "v47", "v48", "v49", "v50", "v51", "v52", "v53", "v54", "v55", "v56", "v57", "v58", "v59", "v60", "v61", "v62", "v63", "v64", "v65", "v66", "v67", "v68", "v69", "v70", "v71", "v72", "v73", "v74", "v75", "v76", "v77", "v78", "v79", "v80", "v81", "v82", "v83", "v84", "v85", "v86", "v87", "v88", "v89", "v90", "v91", "v92", "v93", "v94", "v95", "v96", "v97", "v98", "v99", "v100", "v101", "v102", "v103", "v104", "v105", "v106", "v107", "v108", "v109", "v110", "v111", "v112", "v113", "v114", "v115", "v116", "v117", "v118", "v119", "v120", "v121", "v122", "v123", "v124", "v125", "v126", "v127", "v128", "v129", "v130", "v131", "v132", "v133", "v134", "v135", "v136", "v137", "v138", "v139", "v140", "v141", "v142", "v143", "v144", "v145", "v146", "v147", "v148", "v149", "v150", "v151", "v152", "v153", "v154", "v155", "v156", "v157", "v158", "v159", "v160", "v161", "v162", "v163", "v164", "v165", "v166", "v167", "v168", "v169", "v170", "v171", "v172", "v173", "v174", "v175", "v176", "v177", "v178", "v179", "v180", "v181", "v182", "v183", "v184", "v185", "v186", "v187", "v188", "v189", "v190", "v191", "v192", "v193", "v194", "v195", "v196", "v197", "v198", "v199", "v200", "v201", "v202", "v203", "v204", "v205", "v206", "v207", "v208", "v209", "v210", "v211", "v212", "v213", "v214", "v215", "v216", "v217", "v218", "v219", "v220", "v221", "v222", "v223", "v224", "v225", "v226", "v227", "v228", "v229", "v230", "v231", "v232", "v233", "v234", "v235", "v236", "v237", "v238", "v239", "v240", "v241", "v242", "v243", "v244", "v245", "v246", "v247", "v248", "v249", "v250", "v251", "v252", "v253", "v254", "v255", "s3", "s4", "s5", "s6", "s7", "s8", "s9", "s10", "s11", "s12", "s13", "s14", "s15", "s16", "s17", "s18", "s19", "s20", "s21", "s22", "s23", "s24", "s25", "s26", "s27", "s28", "s29", "s30", "s31", "s33", "s34", "s35", "s36", "s37", "s38", "s39", "s40", "s41", "s42", "s43", "s44", "s45", "s46", "s47", "s48", "s49", "s50", "s51", "s52", "s53", "s54", "s55", "s56", "s57", "s58", "s59", "s60", "s61", "s62", "s63", "s64", "s65", "s66", "s67", "s68", "s69", "s70", "s71", "s72", "s73", "s74", "s75", "s76", "s77", "s78", "s79", "vcc", "scc", "memory");
}

extern "C" void kernel_launch(void* const* d_in, const int* in_sizes, int n_in, void* d_out, int out_size, void* d_ws, size_t ws_size, hipStream_t stream) {
    (void)hipFuncSetAttribute((const void*)fused_attn_block, hipFuncAttributeMaxDynamicSharedMemorySize, 163840);
    fused_attn_block<<<256, 512, 163840, stream>>>(d_in[0], d_in[1], d_in[2], d_in[3], d_in[4], d_in[5], d_out, d_ws);
}
```
